```python
import torch

def _softmax(x, axis=-1):
    return torch.softmax(x, dim=axis)
def _gelu(x):
    return torch.nn.functional.gelu(x, approximate="tanh")
def _silu(x):
    return torch.nn.functional.silu(x)
def _sigmoid(x):
    return torch.sigmoid(x)
def _layernorm(x, axis=-1, eps=1e-5):
    m = x.mean(dim=axis, keepdim=True)
    v = x.var(dim=axis, keepdim=True, unbiased=False)
    return (x - m) / torch.sqrt(v + eps)
def _scatter_add(tgt, idx, src, axis=0):
    return tgt.clone().index_add_(axis, idx, src)
def _segment_sum(src, seg, n):
    out = torch.zeros((n,) + src.shape[1:], dtype=src.dtype)
    return out.index_add_(0, seg, src)
def _pad(x, pw):
    flat = tuple(v for pair in reversed(pw) for v in pair)
    return torch.nn.functional.pad(x, flat)

kernel_name = 'concat_heads__sliding_block__sliding_local_dilated__prompt'

def setup_inputs():

    _w = (lambda a: (lambda t: t.to(torch.bfloat16) if t.is_floating_point() else t)(torch.as_tensor(a)))
    def _A_setup():
        B, S, D = 1, 2048, 448
        return {
            "x": _w((lambda *s: torch.randn(*s, dtype=torch.bfloat16) * 0.2)(B, S, D)),
            "wqkv": _w((lambda *s: torch.randn(*s, dtype=torch.bfloat16) * 0.4)(D, 3 * D)),
            "wo": _w((lambda *s: torch.randn(*s, dtype=torch.bfloat16) * 0.4)(D, D)),
        }
    _A_in = _A_setup()

    def _B_setup():
        B, S, D = 1, 2048, 448
        return {
            "x": _w((lambda *s: torch.randn(*s, dtype=torch.bfloat16) * 0.2)(B, S, D)),
            "wqkv": _w((lambda *s: torch.randn(*s, dtype=torch.bfloat16) * 0.4)(D, 3 * D)),
            "wo": _w((lambda *s: torch.randn(*s, dtype=torch.bfloat16) * 0.4)(D, D)),
        }
    _B_in = _B_setup()

    return {
        "A_x": _A_in["x"],
        "A_wqkv": _A_in["wqkv"],
        "A_wo": _A_in["wo"],
        "B_x": _B_in["x"],
        "B_wqkv": _B_in["wqkv"],
        "B_wo": _B_in["wo"],
    }

def reference(A_x, A_wqkv, A_wo, B_x, B_wqkv, B_wo):
    def _A_ref(x, wqkv, wo):
        B, S, D = x.shape
        H = 16; Dh = D // H; W = 256
        qkv = (lambda x, *d: x.permute(*d))((x @ wqkv).reshape(B, S, 3, H, Dh), 2, 0, 3, 1, 4)
        q, k, v = qkv[0], qkv[1], qkv[2]
        scores = torch.einsum("bhqd,bhkd->bhqk", q, k) * (Dh ** -0.5)
        idx = torch.arange(S)
        mask = (idx[:, None] - idx[None, :] >= 0) & (idx[:, None] - idx[None, :] < W)
        scores = torch.where(mask[None, None], scores, -1e9)
        p = _softmax(scores, axis=-1)
        o = torch.einsum("bhqk,bhkd->bhqd", p, v)
        return (lambda x, *d: x.permute(*d))(o, 0, 2, 1, 3).reshape(B, S, D) @ wo

    def _B_ref(x, wqkv, wo):
        B, S, D = x.shape
        H = 16; Dh = D // H
        W, dil = 8, 2
        qkv = (lambda x, *d: x.permute(*d))((x @ wqkv).reshape(B, S, 3, H, Dh), 2, 0, 3, 1, 4)
        q, k, v = qkv[0], qkv[1], qkv[2]
        scale = Dh ** -0.5
        pos = torch.arange(S)
        off = torch.arange(W) * dil
        idx = pos[:, None] - off[None, :]
        mask = idx >= 0
        idx_c = torch.where(mask, idx, 0).reshape(-1)
        kg = k[:, :, idx_c, :].reshape(B, H, S, W, Dh)
        vg = v[:, :, idx_c, :].reshape(B, H, S, W, Dh)
        logits = torch.einsum("bhsd,bhswd->bhsw", q, kg) * scale
        logits = torch.where(mask[None, None, :, :], logits, -1e9)
        attn = _softmax(logits, axis=-1)
        o = torch.einsum("bhsw,bhswd->bhsd", attn, vg)
        return (lambda x, *d: x.permute(*d))(o, 0, 2, 1, 3).reshape(B, S, D) @ wo

    a = _A_ref(A_x, A_wqkv, A_wo)
    b = _B_ref(B_x, B_wqkv, B_wo)
    return torch.concatenate([a, b], axis=-1)
```

```cpp
#include <hip/hip_runtime.h>
#include <hip/hip_bf16.h>

typedef unsigned short u16;
__device__ __forceinline__ float bf2f(u16 v) { return __uint_as_float(((unsigned)v) << 16); }
__device__ __forceinline__ u16 f2bf(float f) { __hip_bfloat16 b = __float2bfloat16(f); return *(u16*)&b; }

constexpr int S = 2048, D = 448, H = 16, DH = 28, N3 = 1344;

__global__ void gemm_naive(const u16* A, const u16* B, u16* C, int N, int K, int ldc, int lda) {
    int n = blockIdx.x * 64 + threadIdx.x;
    int m = blockIdx.y;
    if (n >= N) return;
    float acc = 0.f;
    for (int k = 0; k < K; ++k) acc += bf2f(A[m * lda + k]) * bf2f(B[k * N + n]);
    C[m * ldc + n] = f2bf(acc);
}

__global__ void attn_naive(const u16* qkv, u16* o, int W, int dil) {
    int s = blockIdx.x * 64 + threadIdx.x;
    int h = blockIdx.y;
    float q[DH], acc[DH];
    for (int d = 0; d < DH; ++d) { q[d] = bf2f(qkv[s * N3 + h * DH + d]); acc[d] = 0.f; }
    float scale = rsqrtf(28.f);
    float m = -1e30f, l = 0.f;
    for (int w = 0; w < W; ++w) {
        int j = s - w * dil;
        if (j < 0) break;
        float sc = 0.f;
        for (int d = 0; d < DH; ++d) sc += q[d] * bf2f(qkv[j * N3 + D + h * DH + d]);
        sc *= scale;
        float mn = fmaxf(m, sc);
        float a = __expf(m - mn), p = __expf(sc - mn);
        l = l * a + p;
        for (int d = 0; d < DH; ++d) acc[d] = acc[d] * a + p * bf2f(qkv[j * N3 + 2 * D + h * DH + d]);
        m = mn;
    }
    for (int d = 0; d < DH; ++d) o[s * D + h * DH + d] = f2bf(acc[d] / l);
}

extern "C" void kernel_launch(void* const* d_in, const int* in_sizes, int n_in,
                              void* d_out, int out_size, void* d_ws, size_t ws_size,
                              hipStream_t stream) {
    u16* ws = (u16*)d_ws;
    u16* out = (u16*)d_out;
    for (int b = 0; b < 2; ++b) {
        const u16* x = (const u16*)d_in[3 * b + 0];
        const u16* wqkv = (const u16*)d_in[3 * b + 1];
        const u16* wo = (const u16*)d_in[3 * b + 2];
        u16* qkv = ws + (size_t)b * S * (N3 + D);
        u16* o = qkv + S * N3;
        gemm_naive<<<dim3(N3 / 64, S), 64, 0, stream>>>(x, wqkv, qkv, N3, D, N3, D);
        attn_naive<<<dim3(S / 64, H), 64, 0, stream>>>(qkv, o, b == 0 ? 256 : 8, b == 0 ? 1 : 2);
        gemm_naive<<<dim3(D / 64, S), 64, 0, stream>>>(o, wo, out + b * D, D, D, 2 * D, D);
    }
}
```

```cpp
#include <hip/hip_runtime.h>
#pragma clang diagnostic ignored "-Wint-to-pointer-cast"

typedef unsigned long long u64;
typedef short bf16x8 __attribute__((ext_vector_type(8)));
typedef short s16x4 __attribute__((ext_vector_type(4)));
typedef float f32x16 __attribute__((ext_vector_type(16)));
typedef float f32x4 __attribute__((ext_vector_type(4)));
typedef unsigned u32x4 __attribute__((ext_vector_type(4)));
typedef unsigned u32x2 __attribute__((ext_vector_type(2)));
typedef __bf16 bf16x2_t __attribute__((ext_vector_type(2)));
typedef float f32x2 __attribute__((ext_vector_type(2)));
#define LAS __attribute__((address_space(3)))
#define GAS __attribute__((address_space(1)))
#define DI __device__ __forceinline__

constexpr int SEQ = 2048, DM = 448;
constexpr int XROW = 896;
constexpr int WROW = 2688;
constexpr int OROW = 1792;

constexpr int L_X = 0;
constexpr int L_W = 49152;
constexpr int L_WQ = 98304;
constexpr int L_MISC = 155648;
constexpr int L_TOTAL = 155648 + 64;
constexpr int L_EXCH = 65536;

constexpr int WS_FLAG = 4096;
constexpr int WS_KV = 65536;
constexpr int WS_O = WS_KV + 2 * 64 * 16 * 4096;
constexpr int WS_END = WS_O + 2 * 64 * 16 * 2048;
#define STAMP(i) do {} while (0)

constexpr unsigned SPIN_CAP = 1u << 18;

#define WAITV_BAR(n) asm volatile("s_waitcnt vmcnt(" #n ") lgkmcnt(0)\n\ts_barrier" ::: "memory")
#define MFMA32(a, b, c) __builtin_amdgcn_mfma_f32_32x32x16_bf16((a), (b), (c), 0, 0, 0)
#define MFMA16(a, b, c) __builtin_amdgcn_mfma_f32_16x16x32_bf16((a), (b), (c), 0, 0, 0)

DI unsigned pk_bf16(float lo, float hi) {
    f32x2 v = {lo, hi};
    bf16x2_t r = __builtin_convertvector(v, bf16x2_t);
    return __builtin_bit_cast(unsigned, r);
}
DI u32x4 pack8(const f32x16& a, int s) {
    u32x4 r;
    r[0] = pk_bf16(a[8 * s + 0], a[8 * s + 1]);
    r[1] = pk_bf16(a[8 * s + 2], a[8 * s + 3]);
    r[2] = pk_bf16(a[8 * s + 4], a[8 * s + 5]);
    r[3] = pk_bf16(a[8 * s + 6], a[8 * s + 7]);
    return r;
}
DI bf16x8 as_frag(u32x4 v) { return __builtin_bit_cast(bf16x8, v); }
DI bf16x8 lds_frag(unsigned addr) { return *(const LAS bf16x8*)addr; }
DI bf16x8 lds_tr2(unsigned a0, unsigned a1) {
    s16x4 lo = __builtin_amdgcn_ds_read_tr16_b64_v4i16((LAS s16x4*)a0);
    s16x4 hi = __builtin_amdgcn_ds_read_tr16_b64_v4i16((LAS s16x4*)a1);
    return __builtin_shufflevector(lo, hi, 0, 1, 2, 3, 4, 5, 6, 7);
}
DI void dma16(u32x4 srd, unsigned voff, unsigned soff, unsigned lds_dst) {
    asm volatile("s_mov_b32 m0, %0\n\ts_nop 0\n\tbuffer_load_dwordx4 %1, %2, %3 offen lds"
                 :: "s"(lds_dst), "v"(voff), "s"(srd), "s"(soff) : "memory");
}
DI u32x4 make_srd(const void* p, unsigned bytes) {
    u64 a = (u64)p;
    u32x4 r = {(unsigned)a, (unsigned)(a >> 32), bytes, 0x00020000u};
    return r;
}
DI u64 ld_agent64(const char* p) { return __hip_atomic_load((GAS u64*)p, __ATOMIC_RELAXED, __HIP_MEMORY_SCOPE_AGENT); }
DI void st_agent64(char* p, u64 v) { __hip_atomic_store((GAS u64*)p, v, __ATOMIC_RELAXED, __HIP_MEMORY_SCOPE_AGENT); }

DI void wait_flags(const char* p, bool act, u64 tag) {
#pragma nounroll
    for (unsigned spins = 0; spins < SPIN_CAP; ++spins) {
        bool ok = true;
        if (act) ok = ld_agent64(p) == tag;
        if (__all(ok)) break;
        __builtin_amdgcn_s_sleep(1);
    }
    asm volatile("" ::: "memory");
}

DI void outproj_tile(const bf16x8 (&wf)[8][4], __amdgpu_buffer_rsrc_t rs_ws, char* out, int ob, int row0, int cb,
                     int wave, int lane) {
    const int m = wave & 3, kh = wave >> 2;
    const int tok0 = row0 + m * 16;
    const unsigned obase = WS_O + ((ob * 64 + (tok0 >> 5)) * 16 + kh * 8) * 2048 + ((tok0 >> 4) & 1) * 1024;
    u32x4 of[8];
#pragma nounroll
    for (unsigned spins = 0; spins < SPIN_CAP; ++spins) {
        asm volatile("" ::: "memory");
        unsigned mx = 0;
#pragma unroll
        for (int j = 0; j < 8; ++j) of[j] = __builtin_amdgcn_raw_buffer_load_b128(rs_ws, lane * 16 + j * 2048, obase, 16);
#pragma unroll
        for (int j = 0; j < 8; ++j) mx = max(max(max(mx, of[j][0]), max(of[j][1], of[j][2])), of[j][3]);
        if (__all(mx != 0xFFFFFFFFu)) break;
        __builtin_amdgcn_s_sleep(1);
    }
    f32x4 acc[4];
#pragma unroll
    for (int c = 0; c < 4; ++c) acc[c] = (f32x4){0.f, 0.f, 0.f, 0.f};
#pragma unroll
    for (int j = 0; j < 8; ++j)
#pragma unroll
        for (int c = 0; c < 4; ++c) acc[c] = MFMA16(wf[j][c], as_frag(of[j]), acc[c]);
    const unsigned ex = L_EXCH + wave * 2048 + lane * 16;
    *(LAS f32x4*)(ex) = kh ? acc[0] : acc[2];
    *(LAS f32x4*)(ex + 1024) = kh ? acc[1] : acc[3];
    asm volatile("s_waitcnt lgkmcnt(0)\n\ts_barrier" ::: "memory");
    const unsigned ey = L_EXCH + (wave ^ 4) * 2048 + lane * 16;
    f32x4 a0 = (kh ? acc[2] : acc[0]) + *(const LAS f32x4*)(ey);
    f32x4 a1 = (kh ? acc[3] : acc[1]) + *(const LAS f32x4*)(ey + 1024);
    char* op = out + (size_t)(tok0 + (lane & 15)) * OROW + ob * XROW + cb * 128 + kh * 64 + (lane >> 4) * 8;
    u32x2 v0 = {pk_bf16(a0[0], a0[1]), pk_bf16(a0[2], a0[3])};
    u32x2 v1 = {pk_bf16(a1[0], a1[1]), pk_bf16(a1[2], a1[3])};
    *(GAS u32x2*)(op) = v0;
    *(GAS u32x2*)(op + 32) = v1;
    asm volatile("s_waitcnt lgkmcnt(0)\n\ts_barrier" ::: "memory");
}

DI void load_wo_frags(bf16x8 (&wf)[8][4], unsigned base, int wave, int lane) {
    const int kh = wave >> 2;
    const int gq = lane >> 4, q = (lane & 15) >> 2, p = lane & 3;
    const int s = gq >> 1, h = gq & 1;
    const unsigned lb = base + (kh * 8 * 28 + 16 * s + 4 * h + q) * 128 + p * 8;
    const int sg = 2 * h + (q >> 1);
    unsigned a0[4], a1[4];
#pragma unroll
    for (int c = 0; c < 4; ++c) {
        a0[c] = lb + ((c ^ sg) * 32);
        a1[c] = a0[c] + (gq == 3 ? 0 : 1024);
    }
#pragma unroll
    for (int j = 0; j < 8; ++j)
#pragma unroll
        for (int c = 0; c < 4; ++c) {
            const int cc = c ^ (2 * (j & 1));
            wf[j][c] = lds_tr2(a0[cc] + j * 3584, a1[cc] + j * 3584);
        }
}

DI void issue_wo(const char* wo, int cb, unsigned lds_base, int wave, int lane) {
    const u32x4 srd = make_srd(wo, DM * XROW);
    const int rr = lane >> 3, pos = lane & 7;
    const unsigned voff = (wave * 8 + rr) * XROW + ((pos ^ (2 * ((rr >> 1) & 3))) * 16);
#pragma unroll
    for (int i = 0; i < 7; ++i) dma16(srd, voff, i * 64 * XROW + cb * 128, lds_base + (i * 8 + wave) * 1024);
}

__global__ void __launch_bounds__(512, 2)
fused_kernel(const char* __restrict__ xa, const char* __restrict__ wa, const char* __restrict__ woa,
             const char* __restrict__ xb, const char* __restrict__ wb, const char* __restrict__ wob,
             char* __restrict__ out, char* __restrict__ ws) {
    extern __shared__ __attribute__((aligned(16))) char smem_[];
    const int wave = __builtin_amdgcn_readfirstlane(threadIdx.x >> 6), lane = threadIdx.x & 63;
    const int xcd = blockIdx.x & 7, idx = blockIdx.x >> 3;
    const int br = xcd >> 2, rt = (xcd & 3) * 4 + (idx & 3), hp = idx >> 2;
    const int tt = wave & 3, hh = wave >> 2;
    const int h = lane >> 5, l31 = lane & 31;
    STAMP(0);

    const u32x4 srd_x = make_srd(br ? xb : xa, SEQ * XROW);
    const u32x4 srd_w = make_srd(br ? wb : wa, DM * WROW);
    const __amdgpu_buffer_rsrc_t rs_ws = __builtin_amdgcn_make_buffer_rsrc(ws, 0, WS_END, 0x00020000);

    const int rr = lane >> 3, pos = lane & 7;
    const unsigned vx0 = (wave * 16 + rr) * XROW + ((pos ^ (rr >> 1)) * 16);
    const unsigned vx1 = (wave * 16 + 8 + rr) * XROW + ((pos ^ (4 + (rr >> 1))) * 16);
    int wc = pos ^ (4 * ((rr >> 1) & 1)); wc = wc == 7 ? 6 : wc;
    const unsigned vw = (wave * 8 + rr) * WROW + wc * 16;
    const int rot = (hp + 2 * (rt & 3)) % 7;
    auto chunk_of = [&](int i) { int c = i + rot; return c >= 7 ? c - 7 : c; };
    auto issue_chunk = [&](int i) {
        const int c = chunk_of(i), b = i % 3;
        const unsigned sx = rt * 128 * XROW + c * 128;
        const unsigned sw = c * 64 * WROW + hp * 112;
        dma16(srd_x, vx0, sx, L_X + b * 16384 + wave * 2048);
        dma16(srd_x, vx1, sx, L_X + b * 16384 + wave * 2048 + 1024);
        dma16(srd_w, vw, sw + XROW, L_W + b * 16384 + wave * 1024);
        dma16(srd_w, vw, sw + 2 * XROW, L_W + b * 16384 + 8192 + wave * 1024);
    };
    auto issue_wq = [&](int i) {
        dma16(srd_w, vw, chunk_of(i) * 64 * WROW + hp * 112, L_WQ + i * 8192 + wave * 1024);
    };
    u64 epoch;
    asm volatile("global_load_dwordx2 %0, %1, %2 sc1" : "=v"(epoch) : "v"(0), "s"(ws) : "memory");
    issue_chunk(0); issue_chunk(1); issue_chunk(2);
    if (threadIdx.x == 0) *(LAS unsigned*)L_MISC = 0u;

    const int xr = tt * 32 + l31;
    unsigned xaddr[4];
#pragma unroll
    for (int ks = 0; ks < 4; ++ks) xaddr[ks] = L_X + xr * 128 + (((2 * ks + h) ^ ((xr >> 1) & 7)) * 16);
    const int blk = (lane >> 4) & 1, q4 = (lane & 15) >> 2, p4 = lane & 3;
    const unsigned woff = (8 * h + q4) * 128 + ((56 * hh + 32 * blk + 8 * p4) ^ (64 * ((q4 >> 1) & 1)));

    f32x16 accK, accV;
#pragma unroll
    for (int i = 0; i < 16; ++i) { accK[i] = 0.f; accV[i] = 0.f; }
    bf16x8 xf[28];
    u64 tag = 0;
#pragma unroll
    for (int i = 0; i < 7; ++i) {
        if (i == 0) WAITV_BAR(8);
        else if (i < 6) WAITV_BAR(4);
        else WAITV_BAR(7);
        if (i == 0) {
            asm volatile("" : "+v"(epoch));
            tag = (((u64)(unsigned)__builtin_amdgcn_readfirstlane((unsigned)(epoch >> 32)) << 32) |
                   (unsigned)__builtin_amdgcn_readfirstlane((unsigned)epoch)) + 1;
        }
        if (i >= 1 && i <= 4) issue_chunk(i + 2);
        if (i == 5) { for (int j = 0; j < 7; ++j) issue_wq(j); }
        const int b = i % 3;
#pragma unroll
        for (int ks = 0; ks < 4; ++ks) {
            xf[i * 4 + ks] = lds_frag(xaddr[ks] + b * 16384);
            const unsigned wa_ = L_W + b * 16384 + woff + ks * 2048;
            const bf16x8 wk = lds_tr2(wa_, wa_ + 512);
            const bf16x8 wv = lds_tr2(wa_ + 8192, wa_ + 8192 + 512);
            accK = MFMA32(wk, xf[i * 4 + ks], accK);
            accV = MFMA32(xf[i * 4 + ks], wv, accV);
        }
    }
    STAMP(1);

    const int g = rt * 4 + tt, head = hp * 2 + hh;
    {
        u32x4 k0 = pack8(accK, 0), k1 = pack8(accK, 1), v0 = pack8(accV, 0), v1 = pack8(accV, 1);
        if (h) { k1[2] = 0u; k1[3] = 0u; }
        if (l31 >= 28) {
            const unsigned f = l31 == 28 ? 0x3F803F80u : 0u;
            v0 = (u32x4){f, f, f, f}; v1 = v0;
        }
        const unsigned la = ((8 + tt) * 2 + hh) * 4096 + lane * 16;
        *(LAS u32x4*)(la) = k0; *(LAS u32x4*)(la + 1024) = k1; *(LAS u32x4*)(la + 2048) = v0; *(LAS u32x4*)(la + 3072) = v1;
        if (br == 0 || tt == 3) {
            const unsigned so = WS_KV + ((br * 64 + g) * 16 + head) * 4096;
            __builtin_amdgcn_raw_buffer_store_b128(k0, rs_ws, lane * 16, so, 16);
            __builtin_amdgcn_raw_buffer_store_b128(k1, rs_ws, lane * 16 + 1024, so, 16);
            __builtin_amdgcn_raw_buffer_store_b128(v0, rs_ws, lane * 16 + 2048, so, 16);
            __builtin_amdgcn_raw_buffer_store_b128(v1, rs_ws, lane * 16 + 3072, so, 16);
        }
        const unsigned oo = WS_O + ((br * 64 + g) * 16 + head) * 2048;
        const u32x4 sent = {0xFFFFFFFFu, 0xFFFFFFFFu, 0xFFFFFFFFu, 0xFFFFFFFFu};
        __builtin_amdgcn_raw_buffer_store_b128(sent, rs_ws, lane * 16, oo, 16);
        __builtin_amdgcn_raw_buffer_store_b128(sent, rs_ws, lane * 16 + 1024, oo, 16);
    }
    if (br == 0 || tt == 3) WAITV_BAR(6); else WAITV_BAR(2);
    STAMP(2);

    f32x16 accQ;
#pragma unroll
    for (int i = 0; i < 16; ++i) accQ[i] = 0.f;
#pragma unroll
    for (int i = 0; i < 28; ++i) {
        const unsigned wa_ = L_WQ + woff + i * 2048;
        accQ = MFMA32(lds_tr2(wa_, wa_ + 512), xf[i], accQ);
        if (i == 17) {
            asm volatile("s_waitcnt vmcnt(0)" ::: "memory");
            unsigned old = 0;
            if (lane == 0) old = __hip_atomic_fetch_add((LAS unsigned*)L_MISC, 1u, __ATOMIC_RELAXED, __HIP_MEMORY_SCOPE_WORKGROUP);
            if (lane == 0 && old == 7) st_agent64(ws + WS_FLAG + ((br * 16 + rt) * 8 + hp) * 128, tag);
        }
    }
    bf16x8 qf[2];
    {
        const float cs = 0.18898223650461363f * 1.4426950408889634f;
#pragma unroll
        for (int i = 0; i < 16; ++i) accQ[i] *= cs;
        u32x4 q0 = pack8(accQ, 0), q1 = pack8(accQ, 1);
        if (h) { q1[2] = 0u; q1[3] = 0xF1000000u; }
        qf[0] = as_frag(q0); qf[1] = as_frag(q1);
    }
    STAMP(3);

    if (br == 0 || wave == 7) {
        const int gp = rt * 4 - 8 + wave;
        u32x4 hv[8];
        if (gp >= 0) {
            wait_flags(ws + WS_FLAG + ((br * 16 + (gp >> 2)) * 8 + hp) * 128, lane == 0, tag);
            const unsigned so = WS_KV + ((br * 64 + gp) * 16 + hp * 2) * 4096;
#pragma unroll
            for (int j = 0; j < 8; ++j) hv[j] = __builtin_amdgcn_raw_buffer_load_b128(rs_ws, lane * 16 + j * 1024, so, 16);
        } else {
#pragma unroll
            for (int j = 0; j < 8; ++j) hv[j] = (u32x4){0u, 0u, 0u, ((j & 3) == 1 && h) ? 0x3F800000u : 0u};
        }
#pragma unroll
        for (int j = 0; j < 8; ++j) *(LAS u32x4*)(wave * 8192 + j * 1024 + lane * 16) = hv[j];
    }
    WAITV_BAR(0);
    STAMP(4);
    if (hp < 7) {
        issue_wo(br ? wob : woa, hp, L_WQ, wave, lane);
        if (br) issue_wo(woa, hp, 0, wave, lane);
    }

    f32x16 accO;
    {
        const unsigned kvb = hh * 4096 + lane * 16;
        const f32x16 zero = {0.f, 0.f, 0.f, 0.f, 0.f, 0.f, 0.f, 0.f, 0.f, 0.f, 0.f, 0.f, 0.f, 0.f, 0.f, 0.f};
        const unsigned own = kvb + (8 + tt) * 8192;
        const unsigned oth = kvb + (br ? 7 + tt : tt) * 8192;
        f32x16 s8 = MFMA32(lds_frag(own), qf[0], zero); s8 = MFMA32(lds_frag(own + 1024), qf[1], s8);
        f32x16 s0 = MFMA32(lds_frag(oth), qf[0], zero); s0 = MFMA32(lds_frag(oth + 1024), qf[1], s0);
        f32x16 sm;
#pragma unroll
        for (int r = 0; r < 16; ++r) {
            const int kr = (r & 3) + 8 * (r >> 2) + 4 * h;
            const int dl = (l31 - kr) & 31;
            float v = kr <= l31 ? s8[r] : s0[r];
            if (br && ((dl & 1) || dl > 14)) v = -3.0e38f;
            sm[r] = v;
        }
        float mx = sm[0];
#pragma unroll
        for (int r = 1; r < 16; ++r) mx = fmaxf(mx, sm[r]);
        f32x16 sf[7];
        if (br == 0) {
#pragma unroll
            for (int u = 0; u < 7; ++u) {
                const unsigned a = kvb + (tt + 1 + u) * 8192;
                sf[u] = MFMA32(lds_frag(a), qf[0], zero); sf[u] = MFMA32(lds_frag(a + 1024), qf[1], sf[u]);
#pragma unroll
                for (int r = 0; r < 16; ++r) mx = fmaxf(mx, sf[u][r]);
            }
        }
        {
            auto sw = __builtin_amdgcn_permlane32_swap(__float_as_uint(mx), __float_as_uint(mx), false, false);
            mx = fmaxf(__uint_as_float(sw[0]), __uint_as_float(sw[1]));
        }
        f32x16 pa, pb;
#pragma unroll
        for (int r = 0; r < 16; ++r) {
            const int kr = (r & 3) + 8 * (r >> 2) + 4 * h;
            const float p = __builtin_amdgcn_exp2f(sm[r] - mx);
            pa[r] = kr <= l31 ? p : 0.f;
            pb[r] = kr <= l31 ? 0.f : p;
        }
        accO = MFMA32(lds_frag(own + 2048), as_frag(pack8(pa, 0)), zero);
        accO = MFMA32(lds_frag(own + 3072), as_frag(pack8(pa, 1)), accO);
        accO = MFMA32(lds_frag(oth + 2048), as_frag(pack8(pb, 0)), accO);
        accO = MFMA32(lds_frag(oth + 3072), as_frag(pack8(pb, 1)), accO);
        if (br == 0) {
#pragma unroll
            for (int u = 0; u < 7; ++u) {
                const unsigned a = kvb + (tt + 1 + u) * 8192;
                f32x16 p;
#pragma unroll
                for (int r = 0; r < 16; ++r) p[r] = __builtin_amdgcn_exp2f(sf[u][r] - mx);
                accO = MFMA32(lds_frag(a + 2048), as_frag(pack8(p, 0)), accO);
                accO = MFMA32(lds_frag(a + 3072), as_frag(pack8(p, 1)), accO);
            }
        }
    }
    {
        auto sw = __builtin_amdgcn_permlane32_swap(__float_as_uint(accO[12]), __float_as_uint(accO[12]), false, false);
        const float den = __uint_as_float(sw[1]);
        const float inv = 1.0f / den;
#pragma unroll
        for (int r = 0; r < 16; ++r) accO[r] *= inv;
        u32x4 o0 = pack8(accO, 0), o1 = pack8(accO, 1);
        if (h) { o1[2] = 0u; o1[3] = 0u; }
#pragma unroll
        for (int j = 0; j < 4; ++j) { o0[j] = min(o0[j], 0xFFFFFFFEu); o1[j] = min(o1[j], 0xFFFFFFFEu); }
        const unsigned oo = WS_O + ((br * 64 + g) * 16 + head) * 2048;
        const unsigned vo = (l31 >> 4) * 1024 + h * 256 + (l31 & 15) * 16;
        __builtin_amdgcn_raw_buffer_store_b128(o0, rs_ws, vo, oo, 16);
        __builtin_amdgcn_raw_buffer_store_b128(o1, rs_ws, vo + 512, oo, 16);
    }
    STAMP(5);

    if (hp == 7) {
        if (br == 1 && rt == 0 && wave == 0) {
#pragma nounroll
            for (int j = 0; j < 4; ++j) wait_flags(ws + WS_FLAG + (j * 64 + lane) * 128, true, tag);
            if (lane == 0) st_agent64(ws, tag);
        }
        return;
    }

    WAITV_BAR(2);
    bf16x8 wf[8][4];
    const int nit = br ? 3 : 1;
#pragma nounroll
    for (int it = 0; it < nit; ++it) {
        const int ob = it == 2 ? 0 : br;
        if (it != 1) {
            load_wo_frags(wf, it == 0 ? L_WQ : 0, wave, lane);
            wait_flags(ws + WS_FLAG + ((ob * 16 + rt) * 8 + (lane & 7)) * 128, lane < 8, tag);
        }
        STAMP(6 + 2 * it);
        outproj_tile(wf, rs_ws, out, ob, rt * 128 + (it ? 64 : 0), hp, wave, lane);
        STAMP(7 + 2 * it);
    }
}

extern "C" void kernel_launch(void* const* d_in, const int* in_sizes, int n_in,
                              void* d_out, int out_size, void* d_ws, size_t ws_size,
                              hipStream_t stream) {
    (void)hipFuncSetAttribute((const void*)fused_kernel, hipFuncAttributeMaxDynamicSharedMemorySize, L_TOTAL);
    fused_kernel<<<256, 512, L_TOTAL, stream>>>((const char*)d_in[0], (const char*)d_in[1], (const char*)d_in[2],
                                                (const char*)d_in[3], (const char*)d_in[4], (const char*)d_in[5],
                                                (char*)d_out, (char*)d_ws);
}
```

```cpp
#define DIAG_SKEW 0
#define DIAG_SEL 0
#include <hip/hip_runtime.h>
#pragma clang diagnostic ignored "-Wint-to-pointer-cast"

typedef unsigned long long u64;
typedef short bf16x8 __attribute__((ext_vector_type(8)));
typedef short s16x4 __attribute__((ext_vector_type(4)));
typedef float f32x16 __attribute__((ext_vector_type(16)));
typedef float f32x4 __attribute__((ext_vector_type(4)));
typedef unsigned u32x4 __attribute__((ext_vector_type(4)));
typedef unsigned u32x2 __attribute__((ext_vector_type(2)));
typedef __bf16 bf16x2_t __attribute__((ext_vector_type(2)));
typedef float f32x2 __attribute__((ext_vector_type(2)));
#define LAS __attribute__((address_space(3)))
#define GAS __attribute__((address_space(1)))
#define DI __device__ __forceinline__

constexpr int SEQ = 2048, DM = 448;
constexpr int XROW = 896;
constexpr int WROW = 2688;
constexpr int OROW = 1792;

constexpr int L_X = 0;
constexpr int L_W = 49152;
constexpr int L_WQ = 98304;
constexpr int L_MISC = 155648;
constexpr int L_TOTAL = 155648 + 64;
constexpr int L_EXCH = 65536;

constexpr int WS_FLAG = 4096;
constexpr int WS_KV = 65536;
constexpr int WS_O = WS_KV + 2 * 64 * 16 * 4096;
constexpr int WS_END = WS_O + 2 * 64 * 16 * 2048;
#define STAMP(i) do {} while (0)

constexpr unsigned SPIN_CAP = 1u << 18;

#define WAITV_BAR(n) asm volatile("s_waitcnt vmcnt(" #n ") lgkmcnt(0)\n\ts_barrier" ::: "memory")
#define MFMA32(a, b, c) __builtin_amdgcn_mfma_f32_32x32x16_bf16((a), (b), (c), 0, 0, 0)
#define MFMA16(a, b, c) __builtin_amdgcn_mfma_f32_16x16x32_bf16((a), (b), (c), 0, 0, 0)

DI unsigned pk_bf16(float lo, float hi) {
    f32x2 v = {lo, hi};
    bf16x2_t r = __builtin_convertvector(v, bf16x2_t);
    return __builtin_bit_cast(unsigned, r);
}
DI u32x4 pack8(const f32x16& a, int s) {
    u32x4 r;
    r[0] = pk_bf16(a[8 * s + 0], a[8 * s + 1]);
    r[1] = pk_bf16(a[8 * s + 2], a[8 * s + 3]);
    r[2] = pk_bf16(a[8 * s + 4], a[8 * s + 5]);
    r[3] = pk_bf16(a[8 * s + 6], a[8 * s + 7]);
    return r;
}
DI bf16x8 as_frag(u32x4 v) { return __builtin_bit_cast(bf16x8, v); }
DI bf16x8 lds_frag(unsigned addr) { return *(const LAS bf16x8*)addr; }
DI bf16x8 lds_tr2(unsigned a0, unsigned a1) {
    s16x4 lo = __builtin_amdgcn_ds_read_tr16_b64_v4i16((LAS s16x4*)a0);
    s16x4 hi = __builtin_amdgcn_ds_read_tr16_b64_v4i16((LAS s16x4*)a1);
    return __builtin_shufflevector(lo, hi, 0, 1, 2, 3, 4, 5, 6, 7);
}
DI void dma16(u32x4 srd, unsigned voff, unsigned soff, unsigned lds_dst) {
    asm volatile("s_mov_b32 m0, %0\n\ts_nop 0\n\tbuffer_load_dwordx4 %1, %2, %3 offen lds"
                 :: "s"(lds_dst), "v"(voff), "s"(srd), "s"(soff) : "memory");
}
DI u32x4 make_srd(const void* p, unsigned bytes) {
    u64 a = (u64)p;
    u32x4 r = {(unsigned)a, (unsigned)(a >> 32), bytes, 0x00020000u};
    return r;
}
DI u64 ld_agent64(const char* p) { return __hip_atomic_load((GAS u64*)p, __ATOMIC_RELAXED, __HIP_MEMORY_SCOPE_AGENT); }
DI void st_agent64(char* p, u64 v) { __hip_atomic_store((GAS u64*)p, v, __ATOMIC_RELAXED, __HIP_MEMORY_SCOPE_AGENT); }

DI void wait_flags(const char* p, bool act, u64 tag) {
#pragma nounroll
    for (unsigned spins = 0; spins < SPIN_CAP; ++spins) {
        bool ok = true;
        if (act) ok = ld_agent64(p) == tag;
        if (__all(ok)) break;
        __builtin_amdgcn_s_sleep(1);
    }
    asm volatile("" ::: "memory");
}

DI void outproj_tile(const bf16x8 (&wf)[8][4], __amdgpu_buffer_rsrc_t rs_ws, char* out, int ob, int row0, int cb,
                     int wave, int lane) {
    const int m = wave & 3, kh = wave >> 2;
    const int tok0 = row0 + m * 16;
    const unsigned obase = WS_O + ((ob * 64 + (tok0 >> 5)) * 16 + kh * 8) * 2048 + ((tok0 >> 4) & 1) * 1024;
    u32x4 of[8];
#pragma nounroll
    for (unsigned spins = 0; spins < SPIN_CAP; ++spins) {
        asm volatile("" ::: "memory");
        unsigned mx = 0;
#pragma unroll
        for (int j = 0; j < 8; ++j) of[j] = __builtin_amdgcn_raw_buffer_load_b128(rs_ws, lane * 16 + j * 2048, obase, 16);
#pragma unroll
        for (int j = 0; j < 8; ++j) mx = max(max(max(mx, of[j][0]), max(of[j][1], of[j][2])), of[j][3]);
        if (__all(mx != 0xFFFFFFFFu)) break;
        __builtin_amdgcn_s_sleep(1);
    }
    f32x4 acc[4];
#pragma unroll
    for (int c = 0; c < 4; ++c) acc[c] = (f32x4){0.f, 0.f, 0.f, 0.f};
#pragma unroll
    for (int j = 0; j < 8; ++j)
#pragma unroll
        for (int c = 0; c < 4; ++c) acc[c] = MFMA16(wf[j][c], as_frag(of[j]), acc[c]);
    const unsigned ex = L_EXCH + wave * 2048 + lane * 16;
    *(LAS f32x4*)(ex) = kh ? acc[0] : acc[2];
    *(LAS f32x4*)(ex + 1024) = kh ? acc[1] : acc[3];
    asm volatile("s_waitcnt lgkmcnt(0)\n\ts_barrier" ::: "memory");
    const unsigned ey = L_EXCH + (wave ^ 4) * 2048 + lane * 16;
    f32x4 a0 = (kh ? acc[2] : acc[0]) + *(const LAS f32x4*)(ey);
    f32x4 a1 = (kh ? acc[3] : acc[1]) + *(const LAS f32x4*)(ey + 1024);
    char* op = out + (size_t)(tok0 + (lane & 15)) * OROW + ob * XROW + cb * 128 + kh * 64 + (lane >> 4) * 8;
    u32x2 v0 = {pk_bf16(a0[0], a0[1]), pk_bf16(a0[2], a0[3])};
    u32x2 v1 = {pk_bf16(a1[0], a1[1]), pk_bf16(a1[2], a1[3])};
    *(GAS u32x2*)(op) = v0;
    *(GAS u32x2*)(op + 32) = v1;
    asm volatile("s_waitcnt lgkmcnt(0)\n\ts_barrier" ::: "memory");
}

DI void load_wo_frags(bf16x8 (&wf)[8][4], unsigned base, int wave, int lane) {
    const int kh = wave >> 2;
    const int gq = lane >> 4, q = (lane & 15) >> 2, p = lane & 3;
    const int s = gq >> 1, h = gq & 1;
    const unsigned lb = base + (kh * 8 * 28 + 16 * s + 4 * h + q) * 128 + p * 8;
    const int sg = 2 * h + (q >> 1);
    unsigned a0[4], a1[4];
#pragma unroll
    for (int c = 0; c < 4; ++c) {
        a0[c] = lb + ((c ^ sg) * 32);
        a1[c] = a0[c] + (gq == 3 ? 0 : 1024);
    }
#pragma unroll
    for (int j = 0; j < 8; ++j)
#pragma unroll
        for (int c = 0; c < 4; ++c) {
            const int cc = c ^ (2 * (j & 1));
            wf[j][c] = lds_tr2(a0[cc] + j * 3584, a1[cc] + j * 3584);
        }
}

DI void issue_wo(const char* wo, int cb, unsigned lds_base, int wave, int lane) {
    const u32x4 srd = make_srd(wo, DM * XROW);
    const int rr = lane >> 3, pos = lane & 7;
    const unsigned voff = (wave * 8 + rr) * XROW + ((pos ^ (2 * ((rr >> 1) & 3))) * 16);
#pragma unroll
    for (int i = 0; i < 7; ++i) dma16(srd, voff, i * 64 * XROW + cb * 128, lds_base + (i * 8 + wave) * 1024);
}

__global__ void __launch_bounds__(512, 2)
fused_kernel(const char* __restrict__ xa, const char* __restrict__ wa, const char* __restrict__ woa,
             const char* __restrict__ xb, const char* __restrict__ wb, const char* __restrict__ wob,
             char* __restrict__ out, char* __restrict__ ws) {
    extern __shared__ __attribute__((aligned(16))) char smem_[];
    const int wave = __builtin_amdgcn_readfirstlane(threadIdx.x >> 6), lane = threadIdx.x & 63;
    const int xcd = blockIdx.x & 7, idx = blockIdx.x >> 3;
    const int br = xcd >> 2, rt = (xcd & 3) * 4 + (idx & 3), hp = idx >> 2;
    const int tt = wave & 3, hh = wave >> 2;
    const int h = lane >> 5, l31 = lane & 31;
    STAMP(0);
    const u64 tstart = __builtin_amdgcn_s_memrealtime();
    if (threadIdx.x == 0) st_agent64(ws + WS_FLAG + 40000 + blockIdx.x * 8, tstart);

    const u32x4 srd_x = make_srd(br ? xb : xa, SEQ * XROW);
    const u32x4 srd_w = make_srd(br ? wb : wa, DM * WROW);
    const __amdgpu_buffer_rsrc_t rs_ws = __builtin_amdgcn_make_buffer_rsrc(ws, 0, WS_END, 0x00020000);

    const int rr = lane >> 3, pos = lane & 7;
    const unsigned vx0 = (wave * 16 + rr) * XROW + ((pos ^ (rr >> 1)) * 16);
    const unsigned vx1 = (wave * 16 + 8 + rr) * XROW + ((pos ^ (4 + (rr >> 1))) * 16);
    int wc = pos ^ (4 * ((rr >> 1) & 1)); wc = wc == 7 ? 6 : wc;
    const unsigned vw = (wave * 8 + rr) * WROW + wc * 16;
    const int rot = (hp + 2 * (rt & 3)) % 7;
    auto chunk_of = [&](int i) { int c = i + rot; return c >= 7 ? c - 7 : c; };
    auto issue_chunk = [&](int i) {
        const int c = chunk_of(i), b = i % 3;
        const unsigned sx = rt * 128 * XROW + c * 128;
        const unsigned sw = c * 64 * WROW + hp * 112;
        dma16(srd_x, vx0, sx, L_X + b * 16384 + wave * 2048);
        dma16(srd_x, vx1, sx, L_X + b * 16384 + wave * 2048 + 1024);
        dma16(srd_w, vw, sw + XROW, L_W + b * 16384 + wave * 1024);
        dma16(srd_w, vw, sw + 2 * XROW, L_W + b * 16384 + 8192 + wave * 1024);
    };
    auto issue_wq = [&](int i) {
        dma16(srd_w, vw, chunk_of(i) * 64 * WROW + hp * 112, L_WQ + i * 8192 + wave * 1024);
    };
    u64 epoch;
    asm volatile("global_load_dwordx2 %0, %1, %2 sc1" : "=v"(epoch) : "v"(0), "s"(ws) : "memory");
    issue_chunk(0); issue_chunk(1); issue_chunk(2);
    if (threadIdx.x == 0) *(LAS unsigned*)L_MISC = 0u;

    const int xr = tt * 32 + l31;
    unsigned xaddr[4];
#pragma unroll
    for (int ks = 0; ks < 4; ++ks) xaddr[ks] = L_X + xr * 128 + (((2 * ks + h) ^ ((xr >> 1) & 7)) * 16);
    const int blk = (lane >> 4) & 1, q4 = (lane & 15) >> 2, p4 = lane & 3;
    const unsigned woff = (8 * h + q4) * 128 + ((56 * hh + 32 * blk + 8 * p4) ^ (64 * ((q4 >> 1) & 1)));

    f32x16 accK, accV;
#pragma unroll
    for (int i = 0; i < 16; ++i) { accK[i] = 0.f; accV[i] = 0.f; }
    bf16x8 xf[28];
    u64 tag = 0;
#pragma unroll
    for (int i = 0; i < 7; ++i) {
        if (i == 0) WAITV_BAR(8);
        else if (i < 6) WAITV_BAR(4);
        else WAITV_BAR(7);
        if (i == 0) {
            asm volatile("" : "+v"(epoch));
            tag = (((u64)(unsigned)__builtin_amdgcn_readfirstlane((unsigned)(epoch >> 32)) << 32) |
                   (unsigned)__builtin_amdgcn_readfirstlane((unsigned)epoch)) + 1;
        }
        if (i >= 1 && i <= 4) issue_chunk(i + 2);
        if (i == 5) { for (int j = 0; j < 7; ++j) issue_wq(j); }
        const int b = i % 3;
#pragma unroll
        for (int ks = 0; ks < 4; ++ks) {
            xf[i * 4 + ks] = lds_frag(xaddr[ks] + b * 16384);
            const unsigned wa_ = L_W + b * 16384 + woff + ks * 2048;
            const bf16x8 wk = lds_tr2(wa_, wa_ + 512);
            const bf16x8 wv = lds_tr2(wa_ + 8192, wa_ + 8192 + 512);
            accK = MFMA32(wk, xf[i * 4 + ks], accK);
            accV = MFMA32(xf[i * 4 + ks], wv, accV);
        }
    }
    STAMP(1);

    const int g = rt * 4 + tt, head = hp * 2 + hh;
    {
        u32x4 k0 = pack8(accK, 0), k1 = pack8(accK, 1), v0 = pack8(accV, 0), v1 = pack8(accV, 1);
        if (h) { k1[2] = 0u; k1[3] = 0u; }
        if (l31 >= 28) {
            const unsigned f = l31 == 28 ? 0x3F803F80u : 0u;
            v0 = (u32x4){f, f, f, f}; v1 = v0;
        }
        const unsigned la = ((8 + tt) * 2 + hh) * 4096 + lane * 16;
        *(LAS u32x4*)(la) = k0; *(LAS u32x4*)(la + 1024) = k1; *(LAS u32x4*)(la + 2048) = v0; *(LAS u32x4*)(la + 3072) = v1;
        if (br == 0 || tt == 3) {
            const unsigned so = WS_KV + ((br * 64 + g) * 16 + head) * 4096;
            __builtin_amdgcn_raw_buffer_store_b128(k0, rs_ws, lane * 16, so, 16);
            __builtin_amdgcn_raw_buffer_store_b128(k1, rs_ws, lane * 16 + 1024, so, 16);
            __builtin_amdgcn_raw_buffer_store_b128(v0, rs_ws, lane * 16 + 2048, so, 16);
            __builtin_amdgcn_raw_buffer_store_b128(v1, rs_ws, lane * 16 + 3072, so, 16);
        }
        const unsigned oo = WS_O + ((br * 64 + g) * 16 + head) * 2048;
        const u32x4 sent = {0xFFFFFFFFu, 0xFFFFFFFFu, 0xFFFFFFFFu, 0xFFFFFFFFu};
        __builtin_amdgcn_raw_buffer_store_b128(sent, rs_ws, lane * 16, oo, 16);
        __builtin_amdgcn_raw_buffer_store_b128(sent, rs_ws, lane * 16 + 1024, oo, 16);
    }
    if (br == 0 || tt == 3) WAITV_BAR(6); else WAITV_BAR(2);
    STAMP(2);

    f32x16 accQ;
#pragma unroll
    for (int i = 0; i < 16; ++i) accQ[i] = 0.f;
#pragma unroll
    for (int i = 0; i < 28; ++i) {
        const unsigned wa_ = L_WQ + woff + i * 2048;
        accQ = MFMA32(lds_tr2(wa_, wa_ + 512), xf[i], accQ);
        if (i == 17) {
            asm volatile("s_waitcnt vmcnt(0)" ::: "memory");
            unsigned old = 0;
            if (lane == 0) old = __hip_atomic_fetch_add((LAS unsigned*)L_MISC, 1u, __ATOMIC_RELAXED, __HIP_MEMORY_SCOPE_WORKGROUP);
            if (lane == 0 && old == 7) st_agent64(ws + WS_FLAG + ((br * 16 + rt) * 8 + hp) * 128, tag);
        }
    }
    bf16x8 qf[2];
    {
        const float cs = 0.18898223650461363f * 1.4426950408889634f;
#pragma unroll
        for (int i = 0; i < 16; ++i) accQ[i] *= cs;
        u32x4 q0 = pack8(accQ, 0), q1 = pack8(accQ, 1);
        if (h) { q1[2] = 0u; q1[3] = 0xF1000000u; }
        qf[0] = as_frag(q0); qf[1] = as_frag(q1);
    }
    STAMP(3);

    if (br == 0 || wave == 7) {
        const int gp = rt * 4 - 8 + wave;
        u32x4 hv[8];
        if (gp >= 0) {
            wait_flags(ws + WS_FLAG + ((br * 16 + (gp >> 2)) * 8 + hp) * 128, lane == 0, tag);
            const unsigned so = WS_KV + ((br * 64 + gp) * 16 + hp * 2) * 4096;
#pragma unroll
            for (int j = 0; j < 8; ++j) hv[j] = __builtin_amdgcn_raw_buffer_load_b128(rs_ws, lane * 16 + j * 1024, so, 16);
        } else {
#pragma unroll
            for (int j = 0; j < 8; ++j) hv[j] = (u32x4){0u, 0u, 0u, ((j & 3) == 1 && h) ? 0x3F800000u : 0u};
        }
#pragma unroll
        for (int j = 0; j < 8; ++j) *(LAS u32x4*)(wave * 8192 + j * 1024 + lane * 16) = hv[j];
    }
    WAITV_BAR(0);
    STAMP(4);
    if (hp < 7) {
        issue_wo(br ? wob : woa, hp, L_WQ, wave, lane);
        if (br) issue_wo(woa, hp, 0, wave, lane);
    }

    f32x16 accO;
    {
        const unsigned kvb = hh * 4096 + lane * 16;
        const f32x16 zero = {0.f, 0.f, 0.f, 0.f, 0.f, 0.f, 0.f, 0.f, 0.f, 0.f, 0.f, 0.f, 0.f, 0.f, 0.f, 0.f};
        const unsigned own = kvb + (8 + tt) * 8192;
        const unsigned oth = kvb + (br ? 7 + tt : tt) * 8192;
        f32x16 s8 = MFMA32(lds_frag(own), qf[0], zero); s8 = MFMA32(lds_frag(own + 1024), qf[1], s8);
        f32x16 s0 = MFMA32(lds_frag(oth), qf[0], zero); s0 = MFMA32(lds_frag(oth + 1024), qf[1], s0);
        f32x16 sm;
#pragma unroll
        for (int r = 0; r < 16; ++r) {
            const int kr = (r & 3) + 8 * (r >> 2) + 4 * h;
            const int dl = (l31 - kr) & 31;
            float v = kr <= l31 ? s8[r] : s0[r];
            if (br && ((dl & 1) || dl > 14)) v = -3.0e38f;
            sm[r] = v;
        }
        float mx = sm[0];
#pragma unroll
        for (int r = 1; r < 16; ++r) mx = fmaxf(mx, sm[r]);
        f32x16 sf[7];
        if (br == 0) {
#pragma unroll
            for (int u = 0; u < 7; ++u) {
                const unsigned a = kvb + (tt + 1 + u) * 8192;
                sf[u] = MFMA32(lds_frag(a), qf[0], zero); sf[u] = MFMA32(lds_frag(a + 1024), qf[1], sf[u]);
#pragma unroll
                for (int r = 0; r < 16; ++r) mx = fmaxf(mx, sf[u][r]);
            }
        }
        {
            auto sw = __builtin_amdgcn_permlane32_swap(__float_as_uint(mx), __float_as_uint(mx), false, false);
            mx = fmaxf(__uint_as_float(sw[0]), __uint_as_float(sw[1]));
        }
        f32x16 pa, pb;
#pragma unroll
        for (int r = 0; r < 16; ++r) {
            const int kr = (r & 3) + 8 * (r >> 2) + 4 * h;
            const float p = __builtin_amdgcn_exp2f(sm[r] - mx);
            pa[r] = kr <= l31 ? p : 0.f;
            pb[r] = kr <= l31 ? 0.f : p;
        }
        accO = MFMA32(lds_frag(own + 2048), as_frag(pack8(pa, 0)), zero);
        accO = MFMA32(lds_frag(own + 3072), as_frag(pack8(pa, 1)), accO);
        accO = MFMA32(lds_frag(oth + 2048), as_frag(pack8(pb, 0)), accO);
        accO = MFMA32(lds_frag(oth + 3072), as_frag(pack8(pb, 1)), accO);
        if (br == 0) {
#pragma unroll
            for (int u = 0; u < 7; ++u) {
                const unsigned a = kvb + (tt + 1 + u) * 8192;
                f32x16 p;
#pragma unroll
                for (int r = 0; r < 16; ++r) p[r] = __builtin_amdgcn_exp2f(sf[u][r] - mx);
                accO = MFMA32(lds_frag(a + 2048), as_frag(pack8(p, 0)), accO);
                accO = MFMA32(lds_frag(a + 3072), as_frag(pack8(p, 1)), accO);
            }
        }
    }
    {
        auto sw = __builtin_amdgcn_permlane32_swap(__float_as_uint(accO[12]), __float_as_uint(accO[12]), false, false);
        const float den = __uint_as_float(sw[1]);
        const float inv = 1.0f / den;
#pragma unroll
        for (int r = 0; r < 16; ++r) accO[r] *= inv;
        u32x4 o0 = pack8(accO, 0), o1 = pack8(accO, 1);
        if (h) { o1[2] = 0u; o1[3] = 0u; }
#pragma unroll
        for (int j = 0; j < 4; ++j) { o0[j] = min(o0[j], 0xFFFFFFFEu); o1[j] = min(o1[j], 0xFFFFFFFEu); }
        const unsigned oo = WS_O + ((br * 64 + g) * 16 + head) * 2048;
        const unsigned vo = (l31 >> 4) * 1024 + h * 256 + (l31 & 15) * 16;
        __builtin_amdgcn_raw_buffer_store_b128(o0, rs_ws, vo, oo, 16);
        __builtin_amdgcn_raw_buffer_store_b128(o1, rs_ws, vo + 512, oo, 16);
    }
    STAMP(5);

    if (hp == 7) {
        if (br == 1 && rt == 0 && wave == 0) {
#pragma nounroll
            for (int j = 0; j < 4; ++j) wait_flags(ws + WS_FLAG + (j * 64 + lane) * 128, true, tag);
            if (lane == 0) st_agent64(ws, tag);
            u64 mn = ~0ull, mx = 0, sel = 0;
            for (int j = 0; j < 256; ++j) { u64 t = ld_agent64(ws + WS_FLAG + 40000 + j * 8); mn = t < mn ? t : mn; mx = t > mx ? t : mx; if (j == DIAG_SEL) sel = t; }
            const u64 te = mn + 20000 + (DIAG_SKEW == 1 ? 10 * (mx - mn) : DIAG_SKEW == 2 ? 10 * (sel - mn) : 0);
            while (__builtin_amdgcn_s_memrealtime() < te) __builtin_amdgcn_s_sleep(1);
        }
        return;
    }

    WAITV_BAR(2);
    bf16x8 wf[8][4];
    const int nit = br ? 3 : 1;
#pragma nounroll
    for (int it = 0; it < nit; ++it) {
        const int ob = it == 2 ? 0 : br;
        if (it != 1) {
            load_wo_frags(wf, it == 0 ? L_WQ : 0, wave, lane);
            wait_flags(ws + WS_FLAG + ((ob * 16 + rt) * 8 + (lane & 7)) * 128, lane < 8, tag);
        }
        STAMP(6 + 2 * it);
        outproj_tile(wf, rs_ws, out, ob, rt * 128 + (it ? 64 : 0), hp, wave, lane);
        STAMP(7 + 2 * it);
    }
}

extern "C" void kernel_launch(void* const* d_in, const int* in_sizes, int n_in,
                              void* d_out, int out_size, void* d_ws, size_t ws_size,
                              hipStream_t stream) {
    (void)hipFuncSetAttribute((const void*)fused_kernel, hipFuncAttributeMaxDynamicSharedMemorySize, L_TOTAL);
    fused_kernel<<<256, 512, L_TOTAL, stream>>>((const char*)d_in[0], (const char*)d_in[1], (const char*)d_in[2],
                                                (const char*)d_in[3], (const char*)d_in[4], (const char*)d_in[5],
                                                (char*)d_out, (char*)d_ws);
}
```

```cpp
#include <hip/hip_runtime.h>
#pragma clang diagnostic ignored "-Wint-to-pointer-cast"

typedef unsigned long long u64;
typedef short bf16x8 __attribute__((ext_vector_type(8)));
typedef short s16x4 __attribute__((ext_vector_type(4)));
typedef float f32x16 __attribute__((ext_vector_type(16)));
typedef float f32x4 __attribute__((ext_vector_type(4)));
typedef unsigned u32x4 __attribute__((ext_vector_type(4)));
typedef __bf16 bf16x2_t __attribute__((ext_vector_type(2)));
typedef float f32x2 __attribute__((ext_vector_type(2)));
#define LAS __attribute__((address_space(3)))
#define GAS __attribute__((address_space(1)))
#define DI __device__ __forceinline__

#define MAP 0
constexpr int SEQ = 2048, DM = 448;
constexpr int XROW = 896;
constexpr int WROW = 2688;
constexpr int OROW = 1792;

constexpr int L_OWN = 65536;
constexpr int L_WQ = 98304;
constexpr int L_Q1 = 155648;
constexpr int L_KEY = 155648;
constexpr int L_TOTAL = 163840;
constexpr int L_EXCH = 65536;

constexpr int WS_FLAG = 4096;
constexpr int WS_KV = 65536;
constexpr int WS_KVB = WS_KV + 8 * 64 * 8192;
constexpr int WS_O = WS_KVB + 8 * 16 * 8192;
constexpr int WS_END = WS_O + 2 * 64 * 16 * 2048;
#define STAMP(i) do {} while (0)
#define STAMP_DECL
#define STAMP_FLUSH

#define B_DELAY 0
constexpr int CODE_BYTES = 20480;
constexpr unsigned SPIN_CAP = 1u << 14;
constexpr unsigned NOWHERE = 0x40000000u;
constexpr u64 UPPER = 0xFFFFFFFF00000000ull;

#define WAITV_BAR(n) asm volatile("s_waitcnt vmcnt(" #n ") lgkmcnt(0)\n\ts_barrier" ::: "memory")
#define BAR() asm volatile("s_waitcnt lgkmcnt(0)\n\ts_barrier" ::: "memory")
#define SCHED() __builtin_amdgcn_sched_barrier(0)
#define MFMA32(a, b, c) __builtin_amdgcn_mfma_f32_32x32x16_bf16((a), (b), (c), 0, 0, 0)
#define LANEMASK(m) __builtin_amdgcn_inverse_ballot_w64(m)

DI unsigned pk_bf16(float lo, float hi) {
    f32x2 v = {lo, hi};
    return __builtin_bit_cast(unsigned, __builtin_convertvector(v, bf16x2_t));
}
DI u32x4 pack8(const f32x16& a, int s) {
    u32x4 r;
    r[0] = pk_bf16(a[8 * s + 0], a[8 * s + 1]);
    r[1] = pk_bf16(a[8 * s + 2], a[8 * s + 3]);
    r[2] = pk_bf16(a[8 * s + 4], a[8 * s + 5]);
    r[3] = pk_bf16(a[8 * s + 6], a[8 * s + 7]);
    return r;
}
DI f32x16 zero16() { return (f32x16){0.f, 0.f, 0.f, 0.f, 0.f, 0.f, 0.f, 0.f, 0.f, 0.f, 0.f, 0.f, 0.f, 0.f, 0.f, 0.f}; }
DI float fmax3(float a, float b, float c) { return __builtin_fmaxf(__builtin_fmaxf(a, b), c); }
DI float sel_mask(u64 m, float t, float f) {
    float r;
    asm("v_cndmask_b32_e64 %0, %1, %2, %3" : "=v"(r) : "v"(f), "v"(t), "s"(m));
    return r;
}
DI bf16x8 as_frag(u32x4 v) { return __builtin_bit_cast(bf16x8, v); }
DI bf16x8 lds_frag(unsigned addr) { return *(const LAS bf16x8*)addr; }
DI bf16x8 lds_tr2(unsigned a0, unsigned a1) {
    s16x4 lo = __builtin_amdgcn_ds_read_tr16_b64_v4i16((LAS s16x4*)a0);
    s16x4 hi = __builtin_amdgcn_ds_read_tr16_b64_v4i16((LAS s16x4*)a1);
    return __builtin_shufflevector(lo, hi, 0, 1, 2, 3, 4, 5, 6, 7);
}
DI unsigned sum4(unsigned m, u32x4 v) { return (m + v[0] + v[1]) + (v[2] + v[3]); }
DI unsigned pair_sum(unsigned s) {
    auto sw = __builtin_amdgcn_permlane32_swap(s, s, false, false);
    return sw[0] + sw[1];
}
DI void dma_pairs(u32x4 srd, unsigned va, unsigned vb, unsigned soff, unsigned step, unsigned lds_dst, unsigned npairs) {
    srd[0] = __builtin_amdgcn_readfirstlane(srd[0]); srd[1] = __builtin_amdgcn_readfirstlane(srd[1]);
    srd[2] = __builtin_amdgcn_readfirstlane(srd[2]); srd[3] = __builtin_amdgcn_readfirstlane(srd[3]);
    soff = __builtin_amdgcn_readfirstlane(soff); step = __builtin_amdgcn_readfirstlane(step);
    lds_dst = __builtin_amdgcn_readfirstlane(lds_dst);
    asm volatile("s_mov_b32 m0, %5\n\t"
                 "s_nop 3\n"
                 "1:\n\t"
                 "buffer_load_dwordx4 %2, %4, %0 offen lds\n\t"
                 "buffer_load_dwordx4 %3, %4, %0 offen offset:1024 lds\n\t"
                 "s_add_u32 m0, m0, 0x800\n\t"
                 "s_add_u32 %0, %0, %6\n\t"
                 "s_sub_u32 %1, %1, 1\n\t"
                 "s_cmp_lg_u32 %1, 0\n\t"
                 "s_cbranch_scc1 1b"
                 : "+s"(soff), "+s"(npairs) : "v"(va), "v"(vb), "s"(srd), "s"(lds_dst), "s"(step) : "memory", "scc");
}
DI u32x4 make_srd(const void* p, unsigned bytes) {
    u64 a = (u64)p;
    u32x4 r = {(unsigned)a, (unsigned)(a >> 32), bytes, 0x00020000u};
    return r;
}
DI u64 ld_agent64(const char* p) { return __hip_atomic_load((GAS u64*)p, __ATOMIC_RELAXED, __HIP_MEMORY_SCOPE_AGENT); }
DI void st_agent64(char* p, u64 v) { __hip_atomic_store((GAS u64*)p, v, __ATOMIC_RELAXED, __HIP_MEMORY_SCOPE_AGENT); }

DI void wait_flags(const char* p, bool act, u64 tag) {
#pragma nounroll
    for (unsigned spins = 0; spins < SPIN_CAP; ++spins) {
        bool ok = true;
        if (act) ok = ld_agent64(p) == tag;
        if (__all(ok)) break;
        __builtin_amdgcn_s_sleep(1);
    }
    asm volatile("" ::: "memory");
}
template <int N> DI void load_pieces(u32x4 (&v)[N], __amdgpu_buffer_rsrc_t rs, unsigned voff, unsigned soff) {
    asm volatile("" ::: "memory");
#pragma unroll
    for (int j = 0; j < N; ++j) v[j] = __builtin_amdgcn_raw_buffer_load_b128(rs, voff + j * 1024, soff, 16);
}
template <int N> DI unsigned sum_pieces(const u32x4 (&v)[N]) {
    unsigned m0 = 0, m1 = 0;
#pragma unroll
    for (int j = 0; j < N; ++j) { m0 = m0 + v[j][0] + v[j][1]; m1 = m1 + v[j][2] + v[j][3]; asm("" : "+v"(m0), "+v"(m1)); }
    return m0 + m1;
}
template <int N> DI bool pieces_ok(const u32x4 (&v)[N], unsigned want) { return __all(pair_sum(sum_pieces(v)) == want); }
template <int N> DI void poll_pieces(u32x4 (&v)[N], __amdgpu_buffer_rsrc_t rs, unsigned voff, unsigned soff, unsigned want) {
    unsigned spins = 0;
#pragma nounroll
    do load_pieces(v, rs, voff, soff); while (!pieces_ok(v, want) && ++spins < SPIN_CAP);
}
template <int N> DI void retry_pieces(u32x4 (&v)[N], __amdgpu_buffer_rsrc_t rs, unsigned voff, unsigned soff, unsigned want) {
    if (__builtin_expect(!pieces_ok(v, want), 0)) poll_pieces(v, rs, voff, soff, want);
}

constexpr u64 own_mask(int r) {
    const int c = (r & 3) + 8 * (r >> 2);
    return (u64)(0xFFFFFFFFu << c) | ((u64)(0xFFFFFFFFu << (c + 4)) << 32);
}
constexpr unsigned dil_half(int k) {
    unsigned m = 0;
    for (int d = 0; d <= 14; d += 2) m |= 1u << ((k + d) & 31);
    return m;
}
constexpr u64 dil_mask(int r) {
    const int c = (r & 3) + 8 * (r >> 2);
    return (u64)dil_half(c) | ((u64)dil_half(c + 4) << 32);
}

__global__ void __launch_bounds__(512, 2)
fused_kernel(const char* __restrict__ xa, const char* __restrict__ wa, const char* __restrict__ woa,
             const char* __restrict__ xb, const char* __restrict__ wb, const char* __restrict__ wob,
             char* __restrict__ out, char* __restrict__ ws) {
    const int wave = __builtin_amdgcn_readfirstlane(threadIdx.x >> 6), lane = threadIdx.x & 63;
    STAMP_DECL
    STAMP(0);
    asm volatile("" :: "s"(xa), "s"(wa), "s"(woa), "s"(xb), "s"(wb), "s"(wob), "s"(out), "s"(ws));
    const int xg = blockIdx.x & 7, br = blockIdx.x >> 7, jj = (blockIdx.x >> 3) & 15;
    const int rt = (xg >> 1) * 4 + (jj & 3), hp = (xg & 1) * 4 + (jj >> 2);
    const int rto = xg * 2 + (jj >= 7 ? 1 : 0), cb = jj >= 7 ? jj - 7 : jj;
    const bool has_out = jj < 14;
    const int rot = (2 * ((jj & 3) + (jj >> 2))) % 7;
    const int pfi = br ? jj : 99;
    const int tt = wave & 3, hh = wave >> 2;
    const int h = lane >> 5, l31 = lane & 31;
    auto chunk_of = [&](int i) { return (int)((0x6543210654321000ull >> (4 * (i + rot + 2))) & 7); };
    const __amdgpu_buffer_rsrc_t rs_ws = __builtin_amdgcn_make_buffer_rsrc(ws, 0, WS_END, 0x00020000);
    const unsigned kv_pub = br ? WS_KVB + (hp * 16 + rt) * 8192 : WS_KV + (hp * 64 + rt * 4 + tt) * 8192;
    const unsigned so1 = rt >= 1 ? kv_pub - (br ? 8192 : 4 * 8192) : NOWHERE;
    const unsigned o_own = WS_O + ((br * 64 + rt * 4) * 16 + hp * 2) * 2048;
    const int rr = lane >> 3, pos = lane & 7;
    const u32x4 srd_wo = make_srd(br ? wob : woa, DM * XROW);
    const unsigned vwo = rr * XROW + ((pos ^ (4 * ((rr >> 1) & 1))) * 16);
    const unsigned so2 = rt >= 2 ? kv_pub - 8 * 8192 : NOWHERE;
    u32x4 q0, q1;
    u32x4 h2[8];
    u64 tag = 0;
    unsigned key;

    f32x16 mD, mF;
    auto make_masks = [&]() {
        float big = -1.0e30f;
        asm volatile("" : "+v"(big));
        if (br == 0) {
#pragma unroll
            for (int r = 0; r < 16; ++r) { mD[r] = sel_mask(own_mask(r), 0.f, big); mF[r] = sel_mask(own_mask(r), big, 0.f); }
        } else {
#pragma unroll
            for (int r = 0; r < 16; ++r) {
                mD[r] = sel_mask(own_mask(r) & dil_mask(r), 0.f, big);
                mF[r] = r < 8 ? big : sel_mask(~own_mask(r) & dil_mask(r), 0.f, big);
            }
        }
        asm volatile("" : "+v"(mD), "+v"(mF));
    };

    if (wave < 4) {
        __builtin_amdgcn_s_setprio(2);
        const int xr = tt * 32 + l31;
        const unsigned xbase = xr * 128 + ((h ^ ((xr >> 1) & 7)) * 16);
        const int blk = (lane >> 4) & 1, q4 = (lane & 15) >> 2, p4 = lane & 3;
        const unsigned wb0 = q4 * 128 + (((blk && p4 == 3) ? 120 : 32 * blk + 8 * p4) ^ (64 * ((q4 >> 1) & 1)));
        const unsigned wb1 = q4 * 128 + ((56 + 32 * blk + 8 * p4) ^ (64 * ((q4 >> 1) & 1)));
        f32x16 aK0, aK1, aV0, aV1;
#pragma unroll
        for (int i = 0; i < 16; ++i) {
            aK0[i] = aK1[i] = (i == 12 && h) ? 1.f : 0.f;
            aV0[i] = aV1[i] = l31 == 28 ? 1.f : 0.f;
        }
        asm volatile("" : "+v"(aK0), "+v"(aK1), "+v"(aV0), "+v"(aV1));
        bf16x8 xf[28];
        bf16x8 wfr[2][4];
        unsigned xa_ = 0, w0_ = 0, w1_ = 0;
        auto rd = [&](int n) {
            const int i = n >> 2, ks = n & 3;
            if (ks == 0) {
                const int lp = (i % 3) * 32768;
                xa_ = xbase + lp; w0_ = wb0 + h * 2048 + lp + 16384; w1_ = wb1 + h * 2048 + lp + 16384;
                asm volatile("" : "+v"(xa_), "+v"(w0_), "+v"(w1_));
                BAR();
            }
            xf[n] = lds_frag(xa_ ^ (ks * 32));
            wfr[n & 1][0] = lds_tr2(w0_ + ks * 4096, w0_ + ks * 4096 + 512);
            wfr[n & 1][1] = lds_tr2(w0_ + ks * 4096 + 1024, w0_ + ks * 4096 + 1536);
            wfr[n & 1][2] = lds_tr2(w1_ + ks * 4096, w1_ + ks * 4096 + 512);
            wfr[n & 1][3] = lds_tr2(w1_ + ks * 4096 + 1024, w1_ + ks * 4096 + 1536);
        };
        rd(0);
#pragma unroll
        for (int n = 0; n < 28; ++n) {
            SCHED();
            const bool edge = n + 1 < 28 && ((n + 1) & 3) == 0;
            if (n + 1 < 28 && !edge) rd(n + 1);
            aK0 = MFMA32(wfr[n & 1][0], xf[n], aK0);
            aV0 = MFMA32(xf[n], wfr[n & 1][1], aV0);
            if (edge) { SCHED(); rd(n + 1); }
            aK1 = MFMA32(wfr[n & 1][2], xf[n], aK1);
            aV1 = MFMA32(xf[n], wfr[n & 1][3], aV1);
            if (n + 1 < 28 && !edge) {
                __builtin_amdgcn_sched_group_barrier(0x008, 1, 0); __builtin_amdgcn_sched_group_barrier(0x102, 4, 0);
                __builtin_amdgcn_sched_group_barrier(0x008, 1, 0); __builtin_amdgcn_sched_group_barrier(0x100, 3, 0);
                __builtin_amdgcn_sched_group_barrier(0x008, 1, 0); __builtin_amdgcn_sched_group_barrier(0x100, 3, 0);
                __builtin_amdgcn_sched_group_barrier(0x008, 1, 0);
            } else if (edge) {
                __builtin_amdgcn_sched_group_barrier(0x100, 3, 0); __builtin_amdgcn_sched_group_barrier(0x008, 1, 0);
                __builtin_amdgcn_sched_group_barrier(0x100, 6, 0); __builtin_amdgcn_sched_group_barrier(0x008, 1, 0);
            }
        }
        SCHED();
        STAMP(1);
        {
            unsigned la = L_OWN + tt * 8192 + lane * 16;
            asm volatile("" : "+v"(la));
            unsigned pv = (br == 0 || tt == 3) ? kv_pub + lane * 16 : NOWHERE;
            asm volatile("" : "+v"(pv));
#pragma unroll
            for (int j = 0; j < 8; ++j) {
                const f32x16& a = j < 2 ? aK0 : j < 4 ? aV0 : j < 6 ? aK1 : aV1;
                const u32x4 v = pack8(a, j & 1);
                *(LAS u32x4*)(la + j * 1024) = v;
                if (j != 5) __builtin_amdgcn_raw_buffer_store_b128(v, rs_ws, pv + j * 1024, 0, 16);
            }
        }
        key = __builtin_amdgcn_readfirstlane(*(const LAS unsigned*)L_KEY);
        BAR();
        STAMP(2);
        f32x16 aQ0, aQ1;
#pragma unroll
        for (int i = 0; i < 16; ++i) { aQ0[i] = 0.f; aQ1[i] = 0.f; }
        bf16x8 qfr[3][2];
        auto rdq = [&](int n) {
            const int i = n >> 2, ks = n & 3;
            if (ks == 0) {
                const unsigned cbase = L_WQ + chunk_of(i) * 8192;
                w0_ = wb0 + h * 1024 + cbase; w1_ = wb1 + h * 1024 + cbase;
                asm volatile("" : "+v"(w0_), "+v"(w1_));
            }
            qfr[n % 3][1] = lds_tr2(w1_ + ks * 2048, w1_ + ks * 2048 + 512);
            qfr[n % 3][0] = lds_tr2(w0_ + ks * 2048, w0_ + ks * 2048 + 512);
        };
        rdq(0); rdq(1);
#pragma unroll
        for (int n = 0; n < 28; ++n) {
            SCHED();
            aQ1 = MFMA32(qfr[n % 3][1], xf[n], aQ1);
            SCHED();
            if (n + 2 < 28) rdq(n + 2);
            SCHED();
            aQ0 = MFMA32(qfr[n % 3][0], xf[n], aQ0);
        }
        SCHED();
        const float cs = 0.18898223650461363f * 1.4426950408889634f;
#pragma unroll
        for (int i = 0; i < 16; ++i) { aQ0[i] *= cs; aQ1[i] *= cs; }
        auto finish_q = [&](const f32x16& aQ, int e, u32x4& f0, u32x4& f1) {
            f0 = pack8(aQ, 0); f1 = pack8(aQ, 1);
            const unsigned ka = L_OWN + tt * 8192 + e * 4096 + lane * 16;
            f32x16 sd = MFMA32(lds_frag(ka), as_frag(f0), zero16());
            sd = MFMA32(lds_frag(ka + 1024), as_frag(f1), sd);
            const float m0 = fmax3(fmax3(sd[0], sd[1], sd[2]), fmax3(sd[3], sd[4], sd[5]), fmax3(sd[6], sd[7], sd[8]));
            const float m1 = fmax3(fmax3(sd[9], sd[10], sd[11]), fmax3(sd[12], sd[13], sd[14]), sd[15]);
            const unsigned sh = pk_bf16(-__builtin_fmaxf(m0, m1), 0.f);
            if (LANEMASK(UPPER)) { f1[2] = sh; f1[3] = 0xF1000000u; }
        };
        u32x4 g0, g1;
        finish_q(aQ1, 1, g0, g1);
        finish_q(aQ0, 0, q0, q1);
        *(LAS u32x4*)(L_Q1 + tt * 2048 + lane * 16) = g0;
        *(LAS u32x4*)(L_Q1 + tt * 2048 + 1024 + lane * 16) = g1;
        STAMP(3);
        make_masks();
        __builtin_amdgcn_s_setprio(0);
    } else {
        if (wave < 7) {
            const int wc = pos ^ (4 * ((rr >> 1) & 1));
            const unsigned vw = wc == 7 ? 0x7FFF0000u : rr * WROW + wc * 16;
            if (wave < 6) {
                const unsigned xm = wave == 4 ? 0xFFFFFFFFu : 0u;
                auto sel = [&](unsigned a, unsigned b) { return (a & xm) | (b & ~xm); };
                const u32x4 srd_a = make_srd(wave == 4 ? xa : wa, sel(SEQ * XROW, DM * WROW));
                const u32x4 srd_b = make_srd(wave == 4 ? xb : wb, sel(SEQ * XROW, DM * WROW));
                const u32x4 srd = br ? srd_b : srd_a;
                const unsigned va = sel(rr * XROW + ((pos ^ (rr >> 1)) * 16), vw + XROW);
                const unsigned vb = sel((8 + rr) * XROW + ((pos ^ (4 + (rr >> 1))) * 16) - 1024, vw + 2 * XROW - 1024);
                const unsigned sbase = sel(rt * 128 * XROW, hp * 112);
                const unsigned cstep = sel(128, 64 * WROW);
                const unsigned pstep = sel(16 * XROW, 8 * WROW);
                const unsigned lsub = sel(0, 16384);
                auto issue = [&](u32x4 s, int i, int slot) { dma_pairs(s, va, vb, sbase + chunk_of(i) * cstep, pstep, slot * 32768 + lsub, 8); };
                if (br && B_DELAY) {
                    const u64 t0 = __builtin_amdgcn_s_memrealtime();
                    while (__builtin_amdgcn_s_memrealtime() - t0 < B_DELAY) __builtin_amdgcn_s_sleep(2);
                }
                issue(srd, 0, 0); issue(srd, 1, 1);
                STAMP(19);
#pragma unroll
                for (int i = 0; i < 5; ++i) {
                    WAITV_BAR(16);
                    STAMP(12 + i);
                    issue(srd, i + 2, (i + 2) % 3);
                }
                WAITV_BAR(16);
                STAMP(17);
                WAITV_BAR(0);
                STAMP(18);
            } else {
                const u32x4 srd_a = make_srd(wa, DM * WROW), srd_b = make_srd(wb, DM * WROW);
                const u32x4 srd = br ? srd_b : srd_a;
                auto issue = [&](u32x4 s, int qi) {
                    const int qq = (qi + jj) & 3;
                    dma_pairs(s, vw, vw + 8 * WROW - 1024, hp * 112 + qq * 112 * WROW, 16 * WROW, L_WQ + qq * 14336, 7);
                };
#pragma unroll
                for (int i = 0; i < 7; ++i) {
                    asm volatile("s_barrier" ::: "memory");
                    if (i == 1) issue(srd, 0);
                    if (i >= 4) issue(srd, i - 3);
                }
            }
        } else {
            unsigned pf = 0;
            u64 epoch;
            asm volatile("global_load_dwordx2 %0, %1, %2 sc1" : "=v"(epoch) : "v"(0), "s"(ws) : "memory");
            if (pfi >= 0 && pfi * 8192 < CODE_BYTES) {
                u64 pc; asm volatile("s_getpc_b64 %0" : "=s"(pc));
                const u32x4 srd_c = make_srd((const void*)(pc & ~127ull), CODE_BYTES);
                asm volatile("buffer_load_dword %0, %1, %2, %3 offen" : "+v"(pf) : "v"(lane * 128), "s"(srd_c), "s"(pfi * 8192) : "memory");
            }
            asm volatile("s_barrier" ::: "memory");
            asm volatile("s_barrier\n\ts_waitcnt vmcnt(0)" : "+v"(epoch) :: "memory");
            tag = (((u64)(unsigned)__builtin_amdgcn_readfirstlane((unsigned)(epoch >> 32)) << 32) |
                   (unsigned)__builtin_amdgcn_readfirstlane((unsigned)epoch)) + 1;
            if (lane == 0) {
                st_agent64(ws + WS_FLAG + blockIdx.x * 128, tag);
                *(LAS unsigned*)L_KEY = (unsigned)tag * 2u + 1u;
            }
            asm volatile("s_waitcnt lgkmcnt(0)\n\ts_barrier\n\ts_barrier\n\ts_barrier\n\ts_barrier\n\ts_barrier\n\ts_waitcnt vmcnt(0)" : "+v"(pf) :: "memory");
        }
        key = __builtin_amdgcn_readfirstlane(*(const LAS unsigned*)L_KEY);
        WAITV_BAR(0);
        STAMP(2);
        if (br == 0 || tt == 3) {
            u32x4 v[8];
            unsigned pv = kv_pub + lane * 16;
            asm volatile("" : "+v"(pv));
#pragma unroll
            for (int j = 0; j < 8; ++j) v[j] = *(const LAS u32x4*)(L_OWN + tt * 8192 + j * 1024 + lane * 16);
            const unsigned m = key - pair_sum(sum_pieces(v));
            if (LANEMASK(UPPER)) v[5][3] = m;
            __builtin_amdgcn_raw_buffer_store_b128(v[5], rs_ws, pv + 5 * 1024, 0, 16);
        } else if (tt != 0 && has_out) {
            dma_pairs(srd_wo, vwo, vwo + 8 * XROW - 1024, cb * 128 + (tt - 1) * 224 * XROW, 16 * XROW, (tt - 1) * 28672, 14);
        }
        if (br && tt == 3 && has_out) {
            const u32x4 srd_a = make_srd(woa, DM * XROW);
            unsigned pf = 0;
#pragma unroll
            for (int j = 0; j < 4; ++j)
                asm volatile("buffer_load_dword %0, %1, %2, %3 offen" : "+v"(pf) : "v"(lane * XROW), "s"(srd_a), "s"(((rto & 1) * 256 + j * 64) * XROW + cb * 128) : "memory");
            asm volatile("s_waitcnt vmcnt(0)" : "+v"(pf) :: "memory");
        }
        STAMP(22);
        make_masks();
        if (br == 0 || tt == 0) {
            const int s1 = br ? 7 : 4 + tt;
            u32x4 h1[8];
            asm volatile("s_waitcnt vmcnt(0)" ::: "memory");
            poll_pieces(h1, rs_ws, lane * 16, so1, rt >= 1 ? key : 0u);
            if (br == 0) load_pieces(h2, rs_ws, lane * 16, so2);
            if (LANEMASK(UPPER)) h1[1][3] = h1[5][3] = rt < 1 ? 0x3F800000u : 0u;
#pragma unroll
            for (int j = 0; j < 8; ++j) *(LAS u32x4*)(s1 * 8192 + j * 1024 + lane * 16) = h1[j];
        }
        STAMP(3);
    }

    {
        unsigned own = L_OWN + hh * 4096 + lane * 16 + tt * 8192;
        unsigned mid = hh * 4096 + lane * 16;
        unsigned far = own - (br ? 8192 : 65536);
        unsigned a6 = mid + (tt > 0 ? 8 : 1) * 8192, a7 = mid + (tt > 1 ? 9 : 2) * 8192, a8 = mid + (tt > 2 ? 10 : 3) * 8192;
        unsigned ov = o_own + tt * 32768 + hh * 2048 + lane * 16;
        asm volatile("" : "+v"(own), "+v"(mid), "+v"(far), "+v"(a6), "+v"(a7), "+v"(a8), "+v"(ov));
        if (wave >= 4) __builtin_amdgcn_s_setprio(1);
        BAR();
        STAMP(4);
        if (wave >= 4) {
            q0 = *(const LAS u32x4*)(L_Q1 + tt * 2048 + lane * 16);
            q1 = *(const LAS u32x4*)(L_Q1 + tt * 2048 + 1024 + lane * 16);
        }
        f32x16 accO;
        if (br == 0) {
            auto ta = [&](int u) { return u == 0 ? own : u <= 4 ? mid + (3 + u) * 8192 : u == 5 ? far : u == 6 ? a6 : u == 7 ? a7 : a8; };
            bf16x8 kf[2][2], vf[2];
            f32x16 sc[2];
            kf[0][0] = lds_frag(ta(0)); kf[0][1] = lds_frag(ta(0) + 1024);
            kf[1][0] = lds_frag(ta(1)); kf[1][1] = lds_frag(ta(1) + 1024);
            vf[0] = lds_frag(ta(0) + 2048); vf[1] = lds_frag(ta(0) + 3072);
            sc[0] = MFMA32(kf[0][0], as_frag(q0), mD); sc[0] = MFMA32(kf[0][1], as_frag(q1), sc[0]);
#pragma unroll
            for (int u = 0; u < 9; ++u) {
                f32x16& p = sc[u & 1];
                f32x16& sn = sc[(u + 1) & 1];
                if (u == 3) {
                    if (wave >= 4) {
                        retry_pieces(h2, rs_ws, lane * 16, so2, rt >= 2 ? key : 0u);
                        if (LANEMASK(UPPER)) h2[1][3] = h2[5][3] = rt < 2 ? 0x3F800000u : 0u;
#pragma unroll
                        for (int j = 0; j < 8; ++j) *(LAS u32x4*)(tt * 8192 + j * 1024 + lane * 16) = h2[j];
                    }
                    __builtin_amdgcn_s_waitcnt(0x0F70);
                    STAMP(20); BAR(); STAMP(21);
                    __builtin_amdgcn_s_setprio(0);
                    if (has_out) dma_pairs(srd_wo, vwo, vwo + 8 * XROW - 1024, cb * 128 + wave * 32 * XROW, 16 * XROW, L_WQ + wave * 4096, 2);
                }
                if (u == 5 && wave < 6 && has_out)
                    dma_pairs(srd_wo, vwo, vwo + 8 * XROW - 1024, cb * 128 + (256 + wave * 32) * XROW, 16 * XROW, L_WQ + 32768 + wave * 4096, 2);
                SCHED();
                if (u < 8) sn = MFMA32(kf[(u + 1) & 1][0], as_frag(q0), u + 1 == 5 ? mF : zero16());
#pragma unroll
                for (int r = 0; r < 4; ++r) p[r] = __builtin_amdgcn_exp2f(p[r]);
                if (u < 7) { kf[u & 1][0] = lds_frag(ta(u + 2)); kf[u & 1][1] = lds_frag(ta(u + 2) + 1024); }
                SCHED();
                if (u < 8) sn = MFMA32(kf[(u + 1) & 1][1], as_frag(q1), sn);
#pragma unroll
                for (int r = 4; r < 10; ++r) p[r] = __builtin_amdgcn_exp2f(p[r]);
                const u32x4 p0 = pack8(p, 0);
                p[10] = __builtin_amdgcn_exp2f(p[10]);
                SCHED();
                accO = MFMA32(vf[0], as_frag(p0), u == 0 ? zero16() : accO);
                if (u < 8) vf[0] = lds_frag(ta(u + 1) + 2048);
#pragma unroll
                for (int r = 11; r < 16; ++r) p[r] = __builtin_amdgcn_exp2f(p[r]);
                const u32x4 p1 = pack8(p, 1);
                SCHED();
                accO = MFMA32(vf[1], as_frag(p1), accO);
                if (u < 8) vf[1] = lds_frag(ta(u + 1) + 3072);
            }
            SCHED();
        } else {
            __builtin_amdgcn_s_waitcnt(0x0F70);
            f32x16 s0 = MFMA32(lds_frag(own), as_frag(q0), mD); s0 = MFMA32(lds_frag(own + 1024), as_frag(q1), s0);
            f32x16 s1 = MFMA32(lds_frag(far), as_frag(q0), mF); s1 = MFMA32(lds_frag(far + 1024), as_frag(q1), s1);
#pragma unroll
            for (int r = 0; r < 16; ++r) s0[r] = __builtin_amdgcn_exp2f(s0[r]);
#pragma unroll
            for (int r = 8; r < 16; ++r) s1[r] = __builtin_amdgcn_exp2f(s1[r]);
            accO = MFMA32(lds_frag(own + 2048), as_frag(pack8(s0, 0)), zero16());
            accO = MFMA32(lds_frag(own + 3072), as_frag(pack8(s0, 1)), accO);
            accO = MFMA32(lds_frag(far + 3072), as_frag(pack8(s1, 1)), accO);
        }
        auto sw = __builtin_amdgcn_permlane32_swap(__float_as_uint(accO[12]), __float_as_uint(accO[12]), false, false);
        float den = __uint_as_float(sw[1]);
        f32x2 c0 = (f32x2){accO[0], accO[1]} + (f32x2){accO[2], accO[3]}, c1 = (f32x2){accO[4], accO[5]} + (f32x2){accO[6], accO[7]};
        f32x2 c2 = (f32x2){accO[8], accO[9]} + (f32x2){accO[10], accO[11]}, c3 = (f32x2){accO[12], accO[13]} + (f32x2){accO[14], accO[15]};
        c0 = (c0 + c1) + (c2 + c3);
        const bool fine = (den < 1.0e9f) && (den > 1.0e-18f) && (__builtin_fabsf(c0[0] + c0[1]) < 3.0e38f);
        const bool redo = !__all(fine);
        if (__builtin_expect(redo, 0)) {
            const int nt = br ? 2 : 9, lim = br ? 14 : 255, par = br ? 1 : 0;
            float mx = -3.0e38f;
            if (LANEMASK(UPPER)) q1[2] = 0u;
#pragma nounroll
            for (int pass = 0; pass < 2; ++pass) {
                accO = zero16();
#pragma nounroll
                for (int t = 0; t < nt; ++t) {
                    const unsigned a = own - t * 8192;
                    f32x16 s = MFMA32(lds_frag(a), as_frag(q0), zero16());
                    s = MFMA32(lds_frag(a + 1024), as_frag(q1), s);
                    float m2 = -3.0e38f;
#pragma unroll
                    for (int r = 0; r < 16; ++r) {
                        const int dist = 32 * t + l31 - ((r & 3) + 8 * (r >> 2) + 4 * h);
                        s[r] = (dist >= 0 && dist <= lim && !(dist & par)) ? s[r] : -3.0e38f;
                        m2 = __builtin_fmaxf(m2, s[r]);
                        s[r] = __builtin_amdgcn_exp2f(s[r] - mx);
                    }
                    if (pass == 0) mx = __builtin_fmaxf(mx, m2);
                    accO = MFMA32(lds_frag(a + 2048), as_frag(pack8(s, 0)), accO);
                    accO = MFMA32(lds_frag(a + 3072), as_frag(pack8(s, 1)), accO);
                }
                auto sm2 = __builtin_amdgcn_permlane32_swap(__float_as_uint(mx), __float_as_uint(mx), false, false);
                mx = __builtin_fmaxf(__uint_as_float(sm2[0]), __uint_as_float(sm2[1]));
            }
            auto sw2 = __builtin_amdgcn_permlane32_swap(__float_as_uint(accO[12]), __float_as_uint(accO[12]), false, false);
            den = __uint_as_float(sw2[1]);
        }
        const float inv = __builtin_amdgcn_rcpf(den);
#pragma unroll
        for (int r = 0; r < 16; ++r) accO[r] *= inv;
        u32x4 o0 = pack8(accO, 0), o1 = pack8(accO, 1);
        if (LANEMASK(UPPER)) { o1[2] = 0u; o1[3] = 0u; }
        __builtin_amdgcn_raw_buffer_store_b128(o0, rs_ws, ov, 0, 16);
        const unsigned ck = key - pair_sum(sum4(0u, o0) + sum4(0u, o1));
        if (LANEMASK(UPPER)) o1[3] = ck;
        __builtin_amdgcn_raw_buffer_store_b128(o1, rs_ws, ov + 1024, 0, 16);
    }
    STAMP(5);

    if (!has_out) {
        if (blockIdx.x == 255 && wave == 7) {
#pragma nounroll
            for (int j = 0; j < 4; ++j) wait_flags(ws + WS_FLAG + (j * 64 + lane) * 128, true, tag);
            if (lane == 0) st_agent64(ws, tag);
        }
        STAMP_FLUSH;
        return;
    }

    WAITV_BAR(0);
    STAMP(6);
    {
        const unsigned ob = WS_O + ((br * 64 + rto * 4 + tt) * 16 + hh * 8) * 2048;
        auto hsel = [&](int j) { return (j + cb) & 7; };
        u32x4 of[8][2];
        load_pieces(of[0], rs_ws, lane * 16, ob + hsel(0) * 2048);
        load_pieces(of[1], rs_ws, lane * 16, ob + hsel(1) * 2048);
        const int rho = ((lane >> 4) & 1) * 4 + (lane & 3), q4 = (lane & 15) >> 2;
        const unsigned lb = (br ? 0 : L_WQ) + (hh * 8 * 28 + 4 * h + q4) * 128 + (((4 * (rho & 1) + (rho >> 1)) * 8) ^ (64 * (((q4 >> 1) & 1) ^ hh)));
        const unsigned padfix = h ? 0 : 1024;
        bf16x8 wf[8][2][2];
        auto rdw = [&](int j) {
            unsigned a = lb + hsel(j) * 3584, a2 = a ^ 64, b = a + padfix, b2 = a2 + padfix;
            asm volatile("" : "+v"(a), "+v"(a2), "+v"(b), "+v"(b2));
            wf[j][0][0] = lds_tr2(a, a + 1024);
            wf[j][0][1] = lds_tr2(a2, a2 + 1024);
            wf[j][1][0] = lds_tr2(a + 2048, b + 2048);
            wf[j][1][1] = lds_tr2(a2 + 2048, b2 + 2048);
        };
        rdw(0); rdw(1); rdw(2);
        STAMP(7);
        if (!__all(pair_sum(sum_pieces(of[0]) + sum_pieces(of[1])) == 2 * key)) {
            unsigned spins = 0;
#pragma nounroll
            do {
                load_pieces(of[0], rs_ws, lane * 16, ob + hsel(0) * 2048);
                load_pieces(of[1], rs_ws, lane * 16, ob + hsel(1) * 2048);
            } while (!__all(pair_sum(sum_pieces(of[0]) + sum_pieces(of[1])) == 2 * key) && ++spins < SPIN_CAP);
        }
        STAMP(8);
#pragma unroll
        for (int j = 2; j < 8; ++j) load_pieces(of[j], rs_ws, lane * 16, ob + hsel(j) * 2048);
        f32x16 acc0, acc1;
#pragma unroll
        for (int j = 0; j < 8; ++j) {
            if (j + 3 < 8) rdw(j + 3);
            if (j >= 2) retry_pieces(of[j], rs_ws, lane * 16, ob + hsel(j) * 2048, key);
            if (LANEMASK(UPPER)) of[j][1][3] = 0u;
            acc1 = MFMA32(wf[j][0][1], as_frag(of[j][0]), j ? acc1 : zero16());
            acc0 = MFMA32(wf[j][0][0], as_frag(of[j][0]), j ? acc0 : zero16());
            acc1 = MFMA32(wf[j][1][1], as_frag(of[j][1]), acc1);
            acc0 = MFMA32(wf[j][1][0], as_frag(of[j][1]), acc0);
        }
        STAMP(9);
        const f32x16 give = acc1;
        f32x16 keep = acc0;
        const unsigned ex = L_EXCH + wave * 4096 + lane * 16;
#pragma unroll
        for (int j = 0; j < 4; ++j) *(LAS f32x4*)(ex + j * 1024) = (f32x4){give[4 * j], give[4 * j + 1], give[4 * j + 2], give[4 * j + 3]};
        BAR();
        const unsigned ey = L_EXCH + (wave ^ 4) * 4096 + lane * 16;
#pragma unroll
        for (int j = 0; j < 4; ++j) {
            const f32x4 t = *(const LAS f32x4*)(ey + j * 1024);
#pragma unroll
            for (int e = 0; e < 4; ++e) keep[4 * j + e] += t[e];
        }
        char* op = out + (size_t)(rto * 128 + tt * 32 + l31) * OROW + br * XROW + cb * 128 + hh * 64 + h * 32;
        *(GAS u32x4*)(op) = pack8(keep, 0);
        *(GAS u32x4*)(op + 16) = pack8(keep, 1);
    }
    STAMP(10);
    STAMP_FLUSH;
}

extern "C" void kernel_launch(void* const* d_in, const int* in_sizes, int n_in,
                              void* d_out, int out_size, void* d_ws, size_t ws_size,
                              hipStream_t stream) {
    (void)hipFuncSetAttribute((const void*)fused_kernel, hipFuncAttributeMaxDynamicSharedMemorySize, L_TOTAL);
    fused_kernel<<<256, 512, L_TOTAL, stream>>>((const char*)d_in[0], (const char*)d_in[1], (const char*)d_in[2],
                                                (const char*)d_in[3], (const char*)d_in[4], (const char*)d_in[5],
                                                (char*)d_out, (char*)d_ws);
}
```

```cpp
#include <hip/hip_runtime.h>
#pragma clang diagnostic ignored "-Wint-to-pointer-cast"

typedef unsigned long long u64;
typedef short bf16x8 __attribute__((ext_vector_type(8)));
typedef short s16x4 __attribute__((ext_vector_type(4)));
typedef float f32x16 __attribute__((ext_vector_type(16)));
typedef float f32x4 __attribute__((ext_vector_type(4)));
typedef unsigned u32x4 __attribute__((ext_vector_type(4)));
typedef __bf16 bf16x2_t __attribute__((ext_vector_type(2)));
typedef float f32x2 __attribute__((ext_vector_type(2)));
#define LAS __attribute__((address_space(3)))
#define GAS __attribute__((address_space(1)))
#define DI __device__ __forceinline__

constexpr int SEQ = 2048, DM = 448;
constexpr int XROW = 896;
constexpr int WROW = 2688;
constexpr int OROW = 1792;

constexpr int L_WQ = 98304;
constexpr int L_JUNK = 155648;
constexpr int L_EPOCH = 155648 + 1024;
constexpr int L_TOTAL = 155648 + 2048;
constexpr int TS = 4096;
constexpr int HS = 12 * TS;

constexpr int WS_FLAG = 4096;
constexpr int WS_KV = 65536;
constexpr int WS_KVB = WS_KV + 16 * 64 * TS;
constexpr int WS_O = WS_KVB + 16 * 16 * TS;
constexpr int WS_END = WS_O + 2 * 64 * 16 * 2048;
#define STAMP(i) do {} while (0)
#define STAMP_DECL
#define STAMP_FLUSH

constexpr int DQ = 16;
constexpr int NPRE = 5;
constexpr unsigned SPIN_CAP = 1u << 16;
constexpr unsigned NOWHERE = 0x40000000u;
constexpr unsigned CKM = 0xBFFFBFFFu;

#define WAITV_BAR(n) asm volatile("s_waitcnt vmcnt(" #n ") lgkmcnt(0)\n\ts_barrier" ::: "memory")
#define BAR() asm volatile("s_waitcnt lgkmcnt(0)\n\ts_barrier" ::: "memory")
#define SCHED() __builtin_amdgcn_sched_barrier(0)
#define MFMA32(a, b, c) __builtin_amdgcn_mfma_f32_32x32x16_bf16((a), (b), (c), 0, 0, 0)
#define WAITV(n) asm volatile("s_waitcnt vmcnt(" #n ")" ::: "memory")
#define AI __attribute__((always_inline))

template <int V> struct IC { static constexpr int value = V; };
template <int I, int N, class F> DI void static_for(F&& f) {
    if constexpr (I < N) { f(IC<I>{}); static_for<I + 1, N>(f); }
}
DI unsigned pk_bf16(float lo, float hi) {
    f32x2 v = {lo, hi};
    return __builtin_bit_cast(unsigned, __builtin_convertvector(v, bf16x2_t));
}
DI u32x4 pack8(const f32x16& a, int s) {
    u32x4 r;
    r[0] = pk_bf16(a[8 * s + 0], a[8 * s + 1]);
    r[1] = pk_bf16(a[8 * s + 2], a[8 * s + 3]);
    r[2] = pk_bf16(a[8 * s + 4], a[8 * s + 5]);
    r[3] = pk_bf16(a[8 * s + 6], a[8 * s + 7]);
    return r;
}
DI f32x16 zero16() { return (f32x16){0.f, 0.f, 0.f, 0.f, 0.f, 0.f, 0.f, 0.f, 0.f, 0.f, 0.f, 0.f, 0.f, 0.f, 0.f, 0.f}; }
DI float dot2(unsigned a, unsigned b, float c) {
    return __builtin_amdgcn_fdot2_f32_bf16(__builtin_bit_cast(bf16x2_t, a), __builtin_bit_cast(bf16x2_t, b), c, false);
}
DI bf16x8 as_frag(u32x4 v) { return __builtin_bit_cast(bf16x8, v); }
DI bf16x8 lds_frag(unsigned addr) { return *(const LAS bf16x8*)addr; }
DI bf16x8 lds_tr2(unsigned a0, unsigned a1) {
    s16x4 lo = __builtin_amdgcn_ds_read_tr16_b64_v4i16((LAS s16x4*)a0);
    s16x4 hi = __builtin_amdgcn_ds_read_tr16_b64_v4i16((LAS s16x4*)a1);
    return __builtin_shufflevector(lo, hi, 0, 1, 2, 3, 4, 5, 6, 7);
}
DI unsigned pair_sum(unsigned s) {
    auto sw = __builtin_amdgcn_permlane32_swap(s, s, false, false);
    return sw[0] + sw[1];
}
DI void dma_pairs(u32x4 srd, unsigned va, unsigned vb, unsigned soff, unsigned step, unsigned lds_dst, unsigned npairs) {
    srd[0] = __builtin_amdgcn_readfirstlane(srd[0]); srd[1] = __builtin_amdgcn_readfirstlane(srd[1]);
    srd[2] = __builtin_amdgcn_readfirstlane(srd[2]); srd[3] = __builtin_amdgcn_readfirstlane(srd[3]);
    soff = __builtin_amdgcn_readfirstlane(soff); step = __builtin_amdgcn_readfirstlane(step);
    lds_dst = __builtin_amdgcn_readfirstlane(lds_dst); npairs = __builtin_amdgcn_readfirstlane(npairs);
    asm volatile("s_mov_b32 m0, %5\n\t"
                 "s_nop 3\n"
                 "1:\n\t"
                 "buffer_load_dwordx4 %2, %4, %0 offen lds\n\t"
                 "buffer_load_dwordx4 %3, %4, %0 offen offset:1024 lds\n\t"
                 "s_add_u32 m0, m0, 0x800\n\t"
                 "s_add_u32 %0, %0, %6\n\t"
                 "s_sub_u32 %1, %1, 1\n\t"
                 "s_cmp_lg_u32 %1, 0\n\t"
                 "s_cbranch_scc1 1b"
                 : "+s"(soff), "+s"(npairs) : "v"(va), "v"(vb), "s"(srd), "s"(lds_dst), "s"(step) : "memory", "scc");
}
DI void dma_pair(u32x4 srd, unsigned va, unsigned vb, unsigned soff, unsigned lds_dst) {
    asm volatile("s_mov_b32 m0, %4\n\ts_nop 0\n\t"
                 "buffer_load_dwordx4 %0, %2, %3 offen lds\n\t"
                 "buffer_load_dwordx4 %1, %2, %3 offen offset:1024 lds"
                 :: "v"(va), "v"(vb), "s"(srd), "s"(soff), "s"(lds_dst) : "memory");
}
DI void dma1(u32x4 srd, unsigned v, unsigned soff, unsigned lds_dst) {
    asm volatile("s_mov_b32 m0, %3\n\ts_nop 0\n\tbuffer_load_dwordx4 %0, %1, %2 offen lds" :: "v"(v), "s"(srd), "s"(soff), "s"(lds_dst) : "memory");
}
DI void dma_tile(u32x4 srd, unsigned v, unsigned soff, unsigned lds_dst) {
    soff = __builtin_amdgcn_readfirstlane(soff); lds_dst = __builtin_amdgcn_readfirstlane(lds_dst);
    asm volatile("s_mov_b32 m0, %3\n\ts_nop 3\n\t"
                 "buffer_load_dwordx4 %0, %1, %2 offen sc1 lds\n\tbuffer_load_dwordx4 %0, %1, %2 offen offset:1024 sc1 lds\n\t"
                 "buffer_load_dwordx4 %0, %1, %2 offen offset:2048 sc1 lds\n\tbuffer_load_dwordx4 %0, %1, %2 offen offset:3072 sc1 lds"
                 :: "v"(v), "s"(srd), "s"(soff), "s"(lds_dst) : "memory");
}
DI void dma_dword(u32x4 srd, unsigned voff, unsigned lds_dst) {
    asm volatile("s_mov_b32 m0, %2\n\ts_nop 0\n\tbuffer_load_dword %0, %1, 0 offen lds" :: "v"(voff), "s"(srd), "s"(lds_dst) : "memory");
}
DI void dma_dword_dev(u32x4 srd, unsigned voff, unsigned lds_dst) {
    asm volatile("s_mov_b32 m0, %2\n\ts_nop 0\n\tbuffer_load_dword %0, %1, 0 offen sc1 lds" :: "v"(voff), "s"(srd), "s"(lds_dst) : "memory");
}
DI u32x4 make_srd(const void* p, unsigned bytes) {
    u64 a = (u64)p;
    u32x4 r = {(unsigned)a, (unsigned)(a >> 32), bytes, 0x00020000u};
    return r;
}
DI u64 ld_agent64(const char* p) { return __hip_atomic_load((GAS u64*)p, __ATOMIC_RELAXED, __HIP_MEMORY_SCOPE_AGENT); }
DI void st_agent64(char* p, u64 v) { __hip_atomic_store((GAS u64*)p, v, __ATOMIC_RELAXED, __HIP_MEMORY_SCOPE_AGENT); }

template <int N> DI void load_pieces(u32x4 (&v)[N], __amdgpu_buffer_rsrc_t rs, unsigned voff, unsigned soff) {
    asm volatile("" ::: "memory");
#pragma unroll
    for (int j = 0; j < N; ++j) v[j] = __builtin_amdgcn_raw_buffer_load_b128(rs, voff + j * 1024, soff, 16);
}
template <int N> DI unsigned sum_pieces(const u32x4 (&v)[N]) {
    unsigned m0 = 0, m1 = 0;
#pragma unroll
    for (int j = 0; j < N; ++j) { m0 = m0 + v[j][0] + v[j][1]; m1 = m1 + v[j][2] + v[j][3]; asm("" : "+v"(m0), "+v"(m1)); }
    return m0 + m1;
}

extern "C" __global__ void __launch_bounds__(768, 3)
fused_kernel(const char* __restrict__ xa, const char* __restrict__ wa, const char* __restrict__ woa,
             const char* __restrict__ xb, const char* __restrict__ wb, const char* __restrict__ wob,
             char* __restrict__ out, char* __restrict__ ws) {
    const int wave = __builtin_amdgcn_readfirstlane(threadIdx.x >> 6), lane = threadIdx.x & 63;
    STAMP_DECL
    STAMP(0);
    {
        u64 pc; unsigned back, len;
        asm volatile("s_getpc_b64 %0\n1:\n\ts_mov_b32 %1, 1b-fused_kernel\n\ts_mov_b32 %2, fused_kernel_end-fused_kernel" : "=s"(pc), "=s"(back), "=s"(len));
        const u32x4 srd_c = make_srd((const void*)(pc - back), (wave == 0 && len < 65536u) ? len : 0);
        dma_dword(srd_c, ((blockIdx.x >> 3) + lane * 32) * 128, L_JUNK);
    }
    const int xg = blockIdx.x & 7, br = blockIdx.x >> 7, jj = (blockIdx.x >> 3) & 15;
    const int rt = (jj & 3) * 4 + (xg >> 1), hp = (xg & 1) * 4 + (jj >> 2);
    const int ro = jj >= 7 ? 1 : 0;
    const int rto = ((xg & 1) * 2 + ro) * 4 + ((xg >> 1) ^ 1), cb = jj - 7 * ro;
    const bool has_out = jj < 14;
    const int h = lane >> 5, l31 = lane & 31;
    const int rr = lane >> 3, pos = lane & 7;
    const int wc = pos ^ (4 * ((rr >> 1) & 1));
    unsigned vw = wc == 7 ? 0x7FFF0000u : rr * WROW + wc * 16;
    unsigned vxa = rr * XROW + ((pos ^ (rr >> 1)) * 16);
    unsigned vxb = (vxa ^ 64) + 8 * XROW - 1024;
    unsigned vwa = vw + XROW, vwb = vw + 2 * XROW - 1024;
    const int wrot = (wave + 2 * jj) & 7;
    unsigned s0x = (rt * 128 + wave * 16) * XROW, s0w = wrot * 8 * WROW + hp * 112;
    asm volatile("" : "+v"(vxa), "+v"(vxb), "+v"(vwa), "+v"(vwb), "+v"(vw), "+s"(s0x), "+s"(s0w));
    asm volatile("" :: "s"(xa), "s"(wa), "s"(woa), "s"(xb), "s"(wb), "s"(wob), "s"(out), "s"(ws));
    const u32x4 srd_x = make_srd(br ? xb : xa, SEQ * XROW), srd_w = make_srd(br ? wb : wa, DM * WROW);
    const u32x4 srd_ws = make_srd(ws, WS_END);
    const __amdgpu_buffer_rsrc_t rs_ws = __builtin_amdgcn_make_buffer_rsrc(ws, 0, WS_END, 0x00020000);
    auto kv_tile = [&](int head, int tile) AI {
        return br ? WS_KVB + ((hp * 2 + head) * 16 + rt) * TS : WS_KV + ((hp * 2 + head) * 64 + rt * 4 + tile) * TS;
    };
    auto l_wo = [&](int half) AI { return br ? half * HS : L_WQ + half * 28672; };

    if (wave >= 8) {
        const int lw = wave - 8, j2 = lw * 2;
        const unsigned sx = (rt * 128 + j2 * 16) * XROW, sw = j2 * 8 * WROW + hp * 112;
        const unsigned vqb = vw + 8 * WROW - 1024;
        auto dx = [&](int c) AI {
            dma_pair(srd_x, vxa, vxb, sx + c * 128, (c % 3) * 32768 + j2 * 2048);
            dma_pair(srd_x, vxa, vxb, sx + c * 128 + 16 * XROW, (c % 3) * 32768 + j2 * 2048 + 2048);
        };
        auto dw = [&](int c) AI {
            dma_pair(srd_w, vwa, vwb, sw + c * 64 * WROW, (c % 3) * 32768 + 16384 + j2 * 2048);
            dma_pair(srd_w, vwa, vwb, sw + (c * 64 + 8) * WROW, (c % 3) * 32768 + 16384 + j2 * 2048 + 2048);
        };
        auto dq = [&](int c) AI { dma_pair(srd_w, vw, vqb, sw + c * 64 * WROW, L_WQ + c * 8192 + j2 * 1024); };
        dma_dword_dev(make_srd(ws, lw == 0 ? 8 : lw == 1 ? WS_END : 0), lw == 0 ? lane * 4 : WS_FLAG + blockIdx.x * 128, L_EPOCH + lw * 256);
        dx(1);
        if (br == 0) dw(1);
        asm volatile("s_barrier" ::: "memory");
        STAMP(12);
        if (br) dw(1);
        dq(1);
        static_for<2, 7>([&](auto cc) AI {
            constexpr int c = decltype(cc)::value;
            dx(c); dw(c);
            if constexpr (c == 2) { dq(c); WAITV_BAR(10); } else WAITV_BAR(8);
        });
#pragma unroll
        for (int q = 0; q < 4; ++q) dq(3 + ((q + jj) & 3));
        WAITV_BAR(8);
        const u32x4 srd_wo = make_srd(br ? wob : woa, has_out ? DM * XROW : 0);
        const unsigned vwo = rr * XROW + wc * 16;
        const u64 tag = (((u64)(unsigned)__builtin_amdgcn_readfirstlane(*(const LAS unsigned*)(L_EPOCH + 4)) << 32) |
                         (unsigned)__builtin_amdgcn_readfirstlane(*(const LAS unsigned*)L_EPOCH)) + 1;
        const unsigned key = (unsigned)tag * 2u + 1u;
        BAR();
        STAMP(2);
        auto wo_part = [&](int b) AI {
            const int e = b ^ ro, p0 = lw < 2 ? lw * 4 : 2 + lw * 3;
            dma_pairs(srd_wo, vwo, vwo + 8 * XROW - 1024, cb * 128 + (e * 14 + p0) * 16 * XROW, 16 * XROW, l_wo(e) + p0 * 2048, lw < 2 ? 4 : 3);
        };
        const int hd = br ? lw : lw >> 1, t0 = br ? 3 : 2 * (lw & 1);
        auto halo_get = [&](int back, int e) AI {
            dma_tile(srd_ws, lane * 16, rt >= back ? kv_tile(hd, t0 + e) - (br ? 1 : 4 * back) * TS : NOWHERE, hd * HS + (8 - 4 * back + t0 + e) * TS);
        };
        auto halo_ok = [&](int back, int e) AI {
            u32x4 hv[4];
#pragma unroll
            for (int j = 0; j < 4; ++j) hv[j] = *(const LAS u32x4*)(hd * HS + (8 - 4 * back + t0 + e) * TS + lane * 16 + j * 1024);
            return __all((((rt >= back ? key : 0u) - pair_sum(sum_pieces(hv))) & CKM) == 0u);
        };
        auto halo_check = [&](int back, int e) AI {
            if (__builtin_expect(!halo_ok(back, e), 0)) {
                unsigned spins = 0;
#pragma nounroll
                do { __builtin_amdgcn_s_sleep(2); halo_get(back, e); WAITV(0); }
                while (!halo_ok(back, e) && ++spins < SPIN_CAP);
            }
            if (__builtin_expect(rt < back, 0) && h) *(LAS unsigned*)(hd * HS + (8 - 4 * back + t0 + e) * TS + lane * 16 + 1024 + 8) = 0x3F800000u;
        };
        __builtin_amdgcn_s_setprio(3);
        WAITV_BAR(0);
        if (lane == 0 && (!br || lw == 3)) st_agent64(ws + WS_FLAG + blockIdx.x * 128 + (3 - lw) * 8, tag);
        if (br == 0) {
            WAITV(0);
            STAMP(22);
            halo_get(1, 0); halo_get(1, 1); halo_get(2, 0); halo_get(2, 1);
            WAITV(12); halo_check(1, 0);
            WAITV(8); halo_check(1, 1);
            STAMP(20);
            BAR();
            WAITV(0); halo_check(2, 0); halo_check(2, 1);
            __builtin_amdgcn_s_setprio(0);
            BAR();
            wo_part(0);
        } else {
            __builtin_amdgcn_s_sleep(4);
            STAMP(22);
            if (lw < 2) { halo_get(1, 0); wo_part(0); WAITV(8); halo_check(1, 0); }
            else { __builtin_amdgcn_s_sleep(2); wo_part(0); }
            __builtin_amdgcn_s_setprio(0);
            STAMP(20);
            BAR();
        }
        wo_part(1);
        WAITV_BAR(0);
        if (blockIdx.x == 255 && lw == 0) {
#pragma nounroll
            for (int j = 0; j < 4; ++j) {
#pragma nounroll
                for (unsigned spins = 0; spins < SPIN_CAP && !__all(ld_agent64(ws + WS_FLAG + (j * 64 + lane) * 128) == tag); ++spins)
                    __builtin_amdgcn_s_sleep(1);
            }
            if (lane == 0) st_agent64(ws, tag);
        }
        STAMP_FLUSH;
        return;
    }

    const int hh = wave >> 2, tt = hh ? 7 - wave : wave;
    auto gather = [&](u32x4 sx, u32x4 sw, int g) AI {
        const int g3 = g >= 6 ? 2 : g >= 3 ? 1 : 0, e = g - 3 * g3, c = 2 * g3 + e;
        const int part = lane >> 4, row = (e == 3 ? 3 + part : c) * 64 + jj * 4 + ((lane >> 2) & 3);
        if (e == 0) dma_dword(sx, (((jj >> 2) * 4 + (xg >> 1)) * 128 + (jj & 3) * 32 + l31) * XROW + (1 + 2 * g3 + h) * 128, L_JUNK);
        else dma_dword(sw, (e < 3 && (part == 3 || (c >= 3 && part == 0))) ? NOWHERE
                       : row * WROW + (e == 3 ? 0 : part) * XROW + ((lane & 3) + (xg & 1) * 3) * 128, L_JUNK);
    };
    if (br) {
        const u32x4 sax = make_srd(xa, SEQ * XROW), saw = make_srd(wa, DM * WROW);
        gather(sax, saw, wave);
        if (wave >= 6) gather(sax, saw, wave + 2);
    }
    dma_pair(srd_x, vxa, vxb, s0x, wave * 2048);
    dma_pair(srd_w, vwa, vwb, s0w, 16384 + wrot * 2048);
    dma1(srd_w, vw, s0w, L_WQ + wrot * 1024);
    if (br) {
        gather(srd_x, srd_w, wave);
        if (wave == 6) gather(srd_x, srd_w, 8);
    }
    STAMP(29);

    f32x16 mD, mF;
    unsigned nD, nF;
    {
        const unsigned upto = (2u << l31) - 1u;
        const unsigned dil = br ? __builtin_rotateleft32(0x5555u, (l31 + 18) & 31) : 0xFFFFFFFFu;
        nD = ~(upto & dil) >> (4 * h); nF = ~(~upto & dil) >> (4 * h);
    }
    auto mask_reg = [&](int r) AI {
        mD[r] = __uint_as_float((unsigned)__builtin_amdgcn_sbfe((int)nD, (r & 3) + 8 * (r >> 2), 1) & 0xF149F2CAu);
        mF[r] = __uint_as_float((unsigned)__builtin_amdgcn_sbfe((int)nF, (r & 3) + 8 * (r >> 2), 1) & 0xF149F2CAu);
        asm volatile("" : "+v"(mD[r]), "+v"(mF[r]));
    };
    unsigned own = hh * HS + (8 + tt) * TS + lane * 16;
    unsigned ov = WS_O + ((br * 64 + rt * 4 + tt) * 16 + hp * 2 + hh) * 2048 + lane * 16;
    unsigned pv = kv_tile(hh, tt) + lane * 16;
    const int xr = tt * 32 + l31;
    const unsigned xbase = xr * 128 + ((h ^ ((xr >> 1) & 7)) * 16);
    unsigned xa_[4], xb_[4];
#pragma unroll
    for (int ks = 0; ks < 4; ++ks) {
        xa_[ks] = xbase ^ (ks * 32); xb_[ks] = xa_[ks] + 65536;
        asm volatile("" : "+v"(xa_[ks]), "+v"(xb_[ks]));
    }
    const int blk = (lane >> 4) & 1, q4 = (lane & 15) >> 2, p4 = lane & 3;
    const unsigned wbo = q4 * 128 + (((!hh && blk && p4 == 3) ? 120 : 56 * hh + 32 * blk + 8 * p4) ^ (64 * ((q4 >> 1) & 1)));
    unsigned wa_ = wbo + h * 2048 + 16384, wc_ = wa_ + 65536, wq_ = wbo + h * 1024 + L_WQ;
    f32x16 aK, aV, aQ;
#pragma unroll
    for (int i = 0; i < 16; ++i) {
        aK[i] = (i == 12 && h) ? 1.f : 0.f;
        aV[i] = l31 == 28 ? 1.f : 0.f;
    }
    asm volatile("" : "+v"(nD), "+v"(nF), "+v"(own), "+v"(ov), "+v"(pv), "+v"(wa_), "+v"(wc_), "+v"(wq_), "+v"(aK), "+v"(aV));
    unsigned lo_ = h ? 0u : 0xFFFFFFFFu;
    asm volatile("" : "+v"(lo_));
    u32x4 q0, q1;
    unsigned key;
    bf16x8 kf[2][2], vf[2];
    {
        constexpr int NQ = 28 - DQ;
        bf16x8 xf[28], fr[2][3], qd[4];
        unsigned keyv = 0;
        auto rd = [&](auto nc) AI {
            constexpr int n = decltype(nc)::value, i = n >> 2, ks = n & 3;
            if constexpr (ks == 0) {
                if constexpr (i == 0) { if (!br) WAITV_BAR(0); else if (wave == 6) WAITV_BAR(2); else WAITV_BAR(1); }
                else BAR();
                if constexpr (i == 6) keyv = *(const LAS unsigned*)L_EPOCH;
            }
            const unsigned wa = (i % 3 == 2 ? wc_ : wa_ + (i % 3) * 32768) + ks * 4096;
            xf[n] = lds_frag(i % 3 == 2 ? xb_[ks] : xa_[ks] + (i % 3) * 32768);
            fr[n & 1][0] = lds_tr2(wa, wa + 512);
            fr[n & 1][1] = lds_tr2(wa + 1024, wa + 1536);
            if constexpr (n < NQ) fr[n & 1][2] = lds_tr2(wq_ + n * 2048, wq_ + n * 2048 + 512);
        };
        rd(IC<0>{});
        static_for<0, 28>([&](auto nc) AI {
            constexpr int n = decltype(nc)::value;
            SCHED();
            aK = MFMA32(fr[n & 1][0], xf[n], aK);
            SCHED();
            if constexpr (n + 1 < 28) rd(IC<n + 1>{});
            else if constexpr (n + 1 == 28) { BAR(); STAMP(2); }
            aV = MFMA32(xf[n], fr[n & 1][1], aV);
            if constexpr (n < NQ) aQ = MFMA32(fr[n & 1][2], xf[n], n ? aQ : zero16());
            if constexpr (n + 1 < 28) {
                __builtin_amdgcn_sched_group_barrier(0x100, 3, 0); __builtin_amdgcn_sched_group_barrier(0x008, 1, 0);
                __builtin_amdgcn_sched_group_barrier(0x100, 3, 0); __builtin_amdgcn_sched_group_barrier(0x008, 1, 0);
                __builtin_amdgcn_sched_group_barrier(0x100, 2, 0);
            }
        });
        SCHED();
        auto rdq = [&](int n) AI { qd[n & 3] = lds_tr2(wq_ + n * 2048, wq_ + n * 2048 + 512); };
        key = __builtin_amdgcn_readfirstlane(keyv) * 2u + 3u;
        u32x4 kv[4];
        kv[0] = pack8(aK, 0); kv[1] = pack8(aK, 1); kv[2] = pack8(aV, 0); kv[3] = pack8(aV, 1);
        if (br == 0 || tt == 3) {
            __builtin_amdgcn_raw_buffer_store_b128(kv[0], rs_ws, pv, 0, 16);
            __builtin_amdgcn_raw_buffer_store_b128(kv[2], rs_ws, pv + 2048, 0, 16);
            __builtin_amdgcn_raw_buffer_store_b128(kv[3], rs_ws, pv + 3072, 0, 16);
            kv[1][3] &= lo_;
            kv[1][3] |= (key - pair_sum(sum_pieces(kv))) & CKM & ~lo_;
            __builtin_amdgcn_raw_buffer_store_b128(kv[1], rs_ws, pv + 1024, 0, 16);
        }
        STAMP(1);
#pragma unroll
        for (int j = 0; j < 4; ++j) *(LAS u32x4*)(own + j * 1024) = kv[j];
        if (br == 0 && tt >= 2) __builtin_amdgcn_s_setprio(1);
        BAR();
        STAMP(4);
        rdq(NQ); rdq(NQ + 1); rdq(NQ + 2);
#pragma unroll
        for (int n = NQ; n < 28; ++n) {
            SCHED();
            aQ = MFMA32(qd[n & 3], xf[n], aQ);
            SCHED();
            if (n + 3 < 28) rdq(n + 3);
            mask_reg(n - NQ);
        }
        STAMP(11);
        const float cs = 0.18898223650461363f * 1.4426950408889634f;
#pragma unroll
        for (int i = 0; i < 16; ++i) aQ[i] *= cs;
        q0 = pack8(aQ, 0); q1 = pack8(aQ, 1);
        kf[0][0] = as_frag(kv[0]); kf[0][1] = as_frag(kv[1]); vf[0] = as_frag(kv[2]); vf[1] = as_frag(kv[3]);
    }

    {
        f32x16 sc[2], accO;
        auto unit = [&](int par, bool first, const f32x16& cin, unsigned ak, unsigned av) AI {
            f32x16& p = sc[par];
            f32x16& sn = sc[par ^ 1];
            u32x4 p0, p1;
            SCHED();
#pragma unroll
            for (int r = 0; r < 4; ++r) p[r] = __builtin_amdgcn_exp2f(p[r]);
            SCHED();
            __builtin_amdgcn_s_waitcnt(0xC17F);
            SCHED();
            sn = MFMA32(kf[par ^ 1][0], as_frag(q0), cin);
            kf[par][0] = lds_frag(ak);
#pragma unroll
            for (int r = 4; r < 8; ++r) p[r] = __builtin_amdgcn_exp2f(p[r]);
            p0[0] = pk_bf16(p[0], p[1]); p0[1] = pk_bf16(p[2], p[3]);
            SCHED();
            sn = MFMA32(kf[par ^ 1][1], as_frag(q1), sn);
            kf[par][1] = lds_frag(ak + 1024);
            p0[2] = pk_bf16(p[4], p[5]); p0[3] = pk_bf16(p[6], p[7]);
            SCHED();
#pragma unroll
            for (int r = 8; r < 12; ++r) p[r] = __builtin_amdgcn_exp2f(p[r]);
            SCHED();
            accO = MFMA32(vf[0], as_frag(p0), first ? zero16() : accO);
            vf[0] = lds_frag(av + 2048);
#pragma unroll
            for (int r = 12; r < 16; ++r) p[r] = __builtin_amdgcn_exp2f(p[r]);
            p1 = pack8(p, 1);
            SCHED();
            accO = MFMA32(vf[1], as_frag(p1), accO);
            vf[1] = lds_frag(av + 3072);
            SCHED();
        };
        const f32x16 z16 = zero16();
        unsigned ob3 = own - 3 * TS;
        asm volatile("" : "+v"(ob3));
        {
            sc[0] = MFMA32(kf[0][0], as_frag(q0), mD);
            const u32x4 k0 = __builtin_bit_cast(u32x4, kf[0][0]), k1 = __builtin_bit_cast(u32x4, kf[0][1]);
            float d0 = 0.f, d1 = 0.f;
#pragma unroll
            for (int j = 0; j < 4; ++j) {
                d0 = dot2(q0[j], k0[j], d0);
                d1 = dot2(q1[j], k1[j], d1);
            }
            d0 += d1;
            auto sd = __builtin_amdgcn_permlane32_swap(__float_as_uint(d0), __float_as_uint(d0), false, false);
            const unsigned sh = pk_bf16(-(__uint_as_float(sd[0]) + __uint_as_float(sd[1])), -6.3e29f);
            q1[2] = (q1[2] & lo_) | (sh & ~lo_);
            sc[0] = MFMA32(kf[0][1], as_frag(q1), sc[0]);
        }
        STAMP(3);
        if (br == 0) {
            kf[1][0] = lds_frag(ob3 + 2 * TS); kf[1][1] = lds_frag(ob3 + 2 * TS + 1024);
            unit(0, true, z16, ob3 + TS, ob3 + 2 * TS);
            if (tt > 0) {
                unit(1, false, z16, ob3, ob3 + TS);
                if (tt > 1) {
                    unit(0, false, z16, ob3, ob3);
                    if (tt > 2) unit(1, false, z16, ob3, ob3);
                }
            }
            STAMP(19);
            unsigned mid = hh * HS + lane * 16 + 4 * TS, far = own - 8 * TS;
            asm volatile("" : "+v"(mid), "+v"(far));
            if (tt < 2) __builtin_amdgcn_s_setprio(2);
            BAR();
            STAMP(21);
            kf[0][0] = lds_frag(mid + 3 * TS); kf[0][1] = lds_frag(mid + 3 * TS + 1024);
            kf[1][0] = lds_frag(mid + 2 * TS); kf[1][1] = lds_frag(mid + 2 * TS + 1024);
            vf[0] = lds_frag(mid + 3 * TS + 2048); vf[1] = lds_frag(mid + 3 * TS + 3072);
            sc[0] = MFMA32(kf[0][0], as_frag(q0), z16); sc[0] = MFMA32(kf[0][1], as_frag(q1), sc[0]);
            unit(0, false, z16, mid + TS, mid + 2 * TS);
            unit(1, false, z16, mid, mid + TS);
            asm volatile("s_barrier" ::: "memory");
            unit(0, false, z16, far, mid);
            unit(1, false, mF, far + TS, far);
            if (tt == 1) __builtin_amdgcn_s_setprio(0);
            unit(0, false, z16, far + 2 * TS, far + TS);
            if (tt < 3) {
                unit(1, false, z16, far + 3 * TS, far + 2 * TS);
                if (tt < 2) {
                    __builtin_amdgcn_s_setprio(0);
                    unit(0, false, z16, far, far + 3 * TS);
                    if (tt < 1) unit(1, false, z16, far, far);
                }
            }
        } else {
#pragma unroll
            for (int r = 0; r < 16; ++r) sc[0][r] = __builtin_amdgcn_exp2f(sc[0][r]);
            accO = MFMA32(vf[0], as_frag(pack8(sc[0], 0)), z16);
            accO = MFMA32(vf[1], as_frag(pack8(sc[0], 1)), accO);
            asm volatile("s_barrier" ::: "memory");
            sc[1] = MFMA32(lds_frag(ob3 + 2 * TS), as_frag(q0), mF);
            sc[1] = MFMA32(lds_frag(ob3 + 2 * TS + 1024), as_frag(q1), sc[1]);
#pragma unroll
            for (int r = 8; r < 16; ++r) sc[1][r] = __builtin_amdgcn_exp2f(sc[1][r]);
            accO = MFMA32(lds_frag(ob3 + 2 * TS + 3072), as_frag(pack8(sc[1], 1)), accO);
        }
        __builtin_amdgcn_s_setprio(0);
        STAMP(23);
        auto sw = __builtin_amdgcn_permlane32_swap(__float_as_uint(accO[12]), __float_as_uint(accO[12]), false, false);
        float den = __uint_as_float(sw[1]);
        u32x4 o0, o1;
        auto finish = [&]() AI {
            const float inv = __builtin_amdgcn_rcpf(den);
#pragma unroll
            for (int r = 0; r < 16; ++r) accO[r] *= inv;
            o0 = pack8(accO, 0);
            __builtin_amdgcn_raw_buffer_store_b128(o0, rs_ws, ov, 0, 16);
            o1 = pack8(accO, 1);
        };
        finish();
        f32x2 c0 = (f32x2){accO[0], accO[1]} + (f32x2){accO[2], accO[3]}, c1 = (f32x2){accO[4], accO[5]} + (f32x2){accO[6], accO[7]};
        f32x2 c2 = (f32x2){accO[8], accO[9]} + (f32x2){accO[10], accO[11]}, c3 = (f32x2){accO[12], accO[13]} + (f32x2){accO[14], accO[15]};
        c0 = (c0 + c1) + (c2 + c3);
        const bool redo = (__ballot(den < 1.0e36f) & __ballot(den > 1.0e-30f) & __ballot(__builtin_fabsf(c0[0] + c0[1]) < 3.0e38f)) != ~0ull;
        if (__builtin_expect(redo, 0)) {
            const int nt = br ? 2 : 9, lim = br ? 14 : 255, par = br ? 1 : 0;
            float mx = -3.0e38f;
            q1[2] &= lo_ | 0xFFFF0000u;
#pragma nounroll
            for (int pass = 0; pass < 2; ++pass) {
                accO = zero16();
#pragma nounroll
                for (int t = 0; t < nt; ++t) {
                    const unsigned a = own - t * TS;
                    f32x16 s = MFMA32(lds_frag(a), as_frag(q0), zero16());
                    s = MFMA32(lds_frag(a + 1024), as_frag(q1), s);
                    float m2 = -3.0e38f;
#pragma unroll
                    for (int r = 0; r < 16; ++r) {
                        const int dist = 32 * t + l31 - ((r & 3) + 8 * (r >> 2) + 4 * h);
                        s[r] = (dist >= 0 && dist <= lim && !(dist & par)) ? s[r] : -3.0e38f;
                        m2 = __builtin_fmaxf(m2, s[r]);
                        s[r] = __builtin_amdgcn_exp2f(s[r] - mx);
                    }
                    if (pass == 0) mx = __builtin_fmaxf(mx, m2);
                    accO = MFMA32(lds_frag(a + 2048), as_frag(pack8(s, 0)), accO);
                    accO = MFMA32(lds_frag(a + 3072), as_frag(pack8(s, 1)), accO);
                }
                auto sm2 = __builtin_amdgcn_permlane32_swap(__float_as_uint(mx), __float_as_uint(mx), false, false);
                mx = __builtin_fmaxf(__uint_as_float(sm2[0]), __uint_as_float(sm2[1]));
            }
            auto sw2 = __builtin_amdgcn_permlane32_swap(__float_as_uint(accO[12]), __float_as_uint(accO[12]), false, false);
            den = __uint_as_float(sw2[1]);
            WAITV(0);
            finish();
        }
        o1[2] &= lo_; o1[3] &= lo_;
        const unsigned ck = key - pair_sum((o0[0] + o0[1] + o0[2]) + (o0[3] + o1[0] + o1[1]) + (o1[2] + o1[3]));
        o1[3] |= ck & ~lo_;
        __builtin_amdgcn_raw_buffer_store_b128(o1, rs_ws, ov + 1024, 0, 16);
    }
    STAMP(5);

    {
        const int to = wave & 3;
        const unsigned ob = WS_O + ((br * 64 + rto * 4 + to) * 16 + hh * 8) * 2048;
        auto hsel = [&](int j) AI { return (j + cb) & 7; };
        const int rho = ((lane >> 4) & 1) * 4 + (lane & 3);
        unsigned lb = l_wo(hh) + (4 * h + q4) * 128 + (((4 * (rho & 1) + (rho >> 1)) * 8) ^ (64 * (((q4 >> 1) & 1) ^ hh)));
        unsigned lb2 = lb ^ 64, lb3 = lb + (lo_ & 1024), lb4 = lb2 + (lo_ & 1024);
        unsigned ex, ey, tw, tq, oo;
        unsigned so[8], sw_[8];
#pragma unroll
        for (int j = 0; j < 8; ++j) { so[j] = ob + hsel(j) * 2048; sw_[j] = hsel(j) * 3584; asm volatile("" : "+s"(so[j]), "+s"(sw_[j])); }
        asm volatile("" : "+v"(lb), "+v"(lb2), "+v"(lb3), "+v"(lb4));
        WAITV_BAR(0);
        if (!has_out) { STAMP_FLUSH; return; }
        STAMP(6);
        u32x4 of[8][2];
        load_pieces(of[0], rs_ws, lane * 16, so[0]);
        load_pieces(of[1], rs_ws, lane * 16, so[1]);
        bf16x8 wf[8][2][2];
        auto rdw = [&](int j) AI {
            unsigned a = lb + sw_[j], a2 = lb2 + sw_[j], b = lb3 + sw_[j], b2 = lb4 + sw_[j];
            asm volatile("" : "+v"(a), "+v"(a2), "+v"(b), "+v"(b2));
            wf[j][0][1] = lds_tr2(a2, a2 + 1024);
            wf[j][1][1] = lds_tr2(a2 + 2048, b2 + 2048);
            wf[j][0][0] = lds_tr2(a, a + 1024);
            wf[j][1][0] = lds_tr2(a + 2048, b + 2048);
        };
#pragma unroll
        for (int j = 0; j < NPRE; ++j) rdw(j);
        STAMP(7);
        auto ld2 = [&](int j) AI {
            load_pieces(of[j], rs_ws, lane * 16, so[j]);
            load_pieces(of[j + 1], rs_ws, lane * 16, so[j + 1]);
        };
        auto ok2 = [&](int j) AI { return __all(pair_sum(sum_pieces(of[j]) + sum_pieces(of[j + 1])) == 2 * key); };
        ld2(2);
        f32x16 acc0, acc1;
#pragma unroll
        for (int g = 0; g < 8; g += 2) {
            if (__builtin_expect(!ok2(g), 0)) {
                unsigned spins = 0;
#pragma nounroll
                do { __builtin_amdgcn_s_sleep(2); ld2(g); } while (!ok2(g) && ++spins < SPIN_CAP);
            }
            if (g == 0) STAMP(8);
            of[g][1][3] &= lo_;
            of[g + 1][1][3] &= lo_;
            if (g < 6) {
#pragma unroll
                for (int j = g; j < g + 2; ++j) {
                    SCHED();
                    acc1 = MFMA32(wf[j][0][1], as_frag(of[j][0]), j ? acc1 : zero16());
                    acc1 = MFMA32(wf[j][1][1], as_frag(of[j][1]), acc1);
                    SCHED();
                    if (j + 4 < 8) load_pieces(of[j + 4], rs_ws, lane * 16, so[j + 4]);
                    SCHED();
                    acc0 = MFMA32(wf[j][0][0], as_frag(of[j][0]), j ? acc0 : zero16());
                    acc0 = MFMA32(wf[j][1][0], as_frag(of[j][1]), acc0);
                    SCHED();
                    if (j + NPRE < 8) rdw(j + NPRE);
                    if (j == 4) {
                        unsigned ln = lane;
                        asm volatile("" : "+v"(ln));
                        const int tr = ln >> 2, r31 = ln & 31;
                        const unsigned eb = (br ? L_WQ : 0) + wave * 4096;
                        ex = eb + ln * 16; ey = ex ^ 16384;
                        tw = (eb ^ 16384) + r31 * 64 + ((2 * (ln >> 5)) ^ ((r31 >> 1) & 3)) * 16;
                        tq = (eb ^ 16384) + tr * 64 + ((ln & 3) ^ ((tr >> 1) & 3)) * 16;
                        oo = (rto * 128 + to * 32 + tr) * OROW + br * XROW + cb * 128 + hh * 64 + (ln & 3) * 16;
                        asm volatile("" : "+v"(ex), "+v"(ey), "+v"(tw), "+v"(tq), "+v"(oo));
                    }
                }
            }
        }
        SCHED();
#pragma unroll
        for (int m = 0; m < 4; ++m) acc1 = MFMA32(wf[6 + (m >> 1)][m & 1][1], as_frag(of[6 + (m >> 1)][m & 1]), acc1);
        acc0 = MFMA32(wf[6][0][0], as_frag(of[6][0]), acc0);
        acc0 = MFMA32(wf[6][1][0], as_frag(of[6][1]), acc0);
        SCHED();
#pragma unroll
        for (int j = 0; j < 4; ++j) {
            *(LAS f32x4*)(ex + j * 1024) = (f32x4){acc1[4 * j], acc1[4 * j + 1], acc1[4 * j + 2], acc1[4 * j + 3]};
            if (j < 2) acc0 = MFMA32(wf[7][j][0], as_frag(of[7][j]), acc0);
            SCHED();
        }
        STAMP(9);
        f32x16 keep = acc0;
        STAMP(13);
        BAR();
        STAMP(14);
#pragma unroll
        for (int j = 0; j < 4; ++j) {
            const f32x4 t = *(const LAS f32x4*)(ey + j * 1024);
#pragma unroll
            for (int e = 0; e < 4; ++e) keep[4 * j + e] += t[e];
        }
        u32x4 r0 = pack8(keep, 0), r1 = pack8(keep, 1);
        asm volatile("" : "+v"(r0), "+v"(r1));
        STAMP(15);
        *(LAS u32x4*)(tw) = r0;
        *(LAS u32x4*)(tw ^ 16) = r1;
        const __amdgpu_buffer_rsrc_t rs_out = __builtin_amdgcn_make_buffer_rsrc(out, 0, SEQ * OROW, 0x00020000);
        __builtin_amdgcn_raw_buffer_store_b128(*(const LAS u32x4*)(tq), rs_out, oo, 0, 0);
        __builtin_amdgcn_raw_buffer_store_b128(*(const LAS u32x4*)(tq + 1024), rs_out, oo, 16 * OROW, 0);
    }
    STAMP(10);
    STAMP_FLUSH;
}

extern "C" __device__ __attribute__((used, noinline)) void fused_kernel_end() {}

extern "C" void kernel_launch(void* const* d_in, const int* in_sizes, int n_in,
                              void* d_out, int out_size, void* d_ws, size_t ws_size,
                              hipStream_t stream) {
    (void)hipFuncSetAttribute((const void*)fused_kernel, hipFuncAttributeMaxDynamicSharedMemorySize, L_TOTAL);
    fused_kernel<<<256, 768, L_TOTAL, stream>>>((const char*)d_in[0], (const char*)d_in[1], (const char*)d_in[2],
                                                (const char*)d_in[3], (const char*)d_in[4], (const char*)d_in[5],
                                                (char*)d_out, (char*)d_ws);
}
```
